```python
import torch

def _softmax(x, axis=-1):
    return torch.softmax(x, dim=axis)
def _gelu(x):
    return torch.nn.functional.gelu(x, approximate="tanh")
def _silu(x):
    return torch.nn.functional.silu(x)
def _sigmoid(x):
    return torch.sigmoid(x)
def _layernorm(x, axis=-1, eps=1e-5):
    m = x.mean(dim=axis, keepdim=True)
    v = x.var(dim=axis, keepdim=True, unbiased=False)
    return (x - m) / torch.sqrt(v + eps)
def _scatter_add(tgt, idx, src, axis=0):
    return tgt.clone().index_add_(axis, idx, src)
def _segment_sum(src, seg, n):
    out = torch.zeros((n,) + src.shape[1:], dtype=src.dtype)
    return out.index_add_(0, seg, src)
def _pad(x, pw):
    flat = tuple(v for pair in reversed(pw) for v in pair)
    return torch.nn.functional.pad(x, flat)

kernel_name = 'standalone__mha_block_cached__near_ridge'

def setup_inputs():

    _w = (lambda a: (lambda t: t.to(torch.bfloat16) if t.is_floating_point() else t)(torch.as_tensor(a)))
    B, Sq, D = 16, 48, 640
    H, Sk, Dh = 32, 48, 192
    return {
        "x": _w((lambda *s: torch.randn(*s, dtype=torch.bfloat16) * 0.8)(B, Sq, D)),
        "k_cache": _w((lambda *s: torch.randn(*s, dtype=torch.bfloat16) * 0.8)(B, H, Sk, Dh)),
        "v_cache": _w((lambda *s: torch.randn(*s, dtype=torch.bfloat16) * 0.05)(B, H, Sk, Dh)),
        "wq": _w((lambda *s: torch.randn(*s, dtype=torch.bfloat16) * 0.8)(D, H * Dh) * (D ** -0.5)),
        "wo": _w((lambda *s: torch.randn(*s, dtype=torch.bfloat16) * 0.05)(H * Dh, D) * ((H * Dh) ** -0.5)),
    }

def reference(x, k_cache, v_cache, wq, wo):
    B, Sq, D = x.shape
    H, Dh = 32, 192
    q = (lambda x, *d: x.permute(*d))((x @ wq).reshape(B, Sq, H, Dh), 0, 2, 1, 3)
    scores = torch.einsum("bhqd,bhkd->bhqk", q, k_cache) * (Dh ** -0.5)
    p = _softmax(scores, axis=-1)
    o = torch.einsum("bhqk,bhkd->bhqd", p, v_cache)
    return (lambda x, *d: x.permute(*d))(o, 0, 2, 1, 3).reshape(B, Sq, H * Dh) @ wo
```

```cpp
#include <hip/hip_runtime.h>
#include <hip/hip_bf16.h>

typedef unsigned short u16;
__device__ __forceinline__ float bf2f(u16 b){ return __uint_as_float(((unsigned)b)<<16); }
__device__ __forceinline__ u16 f2bf(float f){ unsigned u=__float_as_uint(f); u += 0x7FFFu + ((u>>16)&1u); return (u16)(u>>16); }

constexpr int B=16,SQ=48,D=640,H=32,SK=48,DH=192,HD=H*DH;

__global__ void k_q(const u16* x,const u16* wq,u16* q){
  int n=blockIdx.x*256+threadIdx.x; int m=blockIdx.y;
  float acc=0;
  for(int k=0;k<D;++k) acc+=bf2f(x[m*D+k])*bf2f(wq[(size_t)k*HD+n]);
  q[(size_t)m*HD+n]=f2bf(acc);
}
__global__ void k_attn(const u16* q,const u16* kc,const u16* vc,u16* o){
  __shared__ float p[SQ][SK];
  int bh=blockIdx.x; int b=bh/H,h=bh%H;
  const u16* K=kc+(size_t)bh*SK*DH; const u16* V=vc+(size_t)bh*SK*DH;
  for(int i=threadIdx.x;i<SQ*SK;i+=blockDim.x){
    int qi=i/SK,ki=i%SK; float acc=0;
    for(int d=0;d<DH;++d) acc+=bf2f(q[(size_t)(b*SQ+qi)*HD+h*DH+d])*bf2f(K[ki*DH+d]);
    p[qi][ki]=acc*0.07216878364870322f;
  }
  __syncthreads();
  if(threadIdx.x<SQ){
    int qi=threadIdx.x; float m=-1e30f; for(int k=0;k<SK;++k) m=fmaxf(m,p[qi][k]);
    float s=0; for(int k=0;k<SK;++k){ float e=__expf(p[qi][k]-m); p[qi][k]=e; s+=e;}
    for(int k=0;k<SK;++k) p[qi][k]/=s;
  }
  __syncthreads();
  for(int i=threadIdx.x;i<SQ*DH;i+=blockDim.x){
    int qi=i/DH,d=i%DH; float acc=0;
    for(int k=0;k<SK;++k) acc+=p[qi][k]*bf2f(V[k*DH+d]);
    o[(size_t)(b*SQ+qi)*HD+h*DH+d]=f2bf(acc);
  }
}
__global__ void k_o(const u16* o,const u16* wo,u16* out){
  int n=blockIdx.x*128+threadIdx.x; int m=blockIdx.y;
  float acc=0;
  for(int k=0;k<HD;++k) acc+=bf2f(o[(size_t)m*HD+k])*bf2f(wo[(size_t)k*D+n]);
  out[(size_t)m*D+n]=f2bf(acc);
}

extern "C" void kernel_launch(void* const* d_in, const int* in_sizes, int n_in,
                              void* d_out, int out_size, void* d_ws, size_t ws_size,
                              hipStream_t stream) {
  const u16* x=(const u16*)d_in[0]; const u16* kc=(const u16*)d_in[1]; const u16* vc=(const u16*)d_in[2];
  const u16* wq=(const u16*)d_in[3]; const u16* wo=(const u16*)d_in[4];
  u16* q=(u16*)d_ws; u16* o=q+(size_t)B*SQ*HD;
  k_q<<<dim3(HD/256,B*SQ),256,0,stream>>>(x,wq,q);
  k_attn<<<B*H,256,0,stream>>>(q,kc,vc,o);
  k_o<<<dim3(D/128,B*SQ),128,0,stream>>>(o,wo,(u16*)d_out);
}
```

```cpp
#include <cstdio>
#include <hip/hip_runtime.h>
#include <hip/hip_bf16.h>

typedef unsigned short u16;
__device__ __forceinline__ float bf2f(u16 b){ return __uint_as_float(((unsigned)b)<<16); }
__device__ __forceinline__ u16 f2bf(float f){ unsigned u=__float_as_uint(f); u += 0x7FFFu + ((u>>16)&1u); return (u16)(u>>16); }

constexpr int B=16,SQ=48,D=640,H=32,SK=48,DH=192,HD=H*DH;

__global__ void k_q(const u16* x,const u16* wq,u16* q){
  int n=blockIdx.x*256+threadIdx.x; int m=blockIdx.y;
  float acc=0;
  for(int k=0;k<D;++k) acc+=bf2f(x[m*D+k])*bf2f(wq[(size_t)k*HD+n]);
  q[(size_t)m*HD+n]=f2bf(acc);
}
__global__ void k_attn(const u16* q,const u16* kc,const u16* vc,u16* o){
  __shared__ float p[SQ][SK];
  int bh=blockIdx.x; int b=bh/H,h=bh%H;
  const u16* K=kc+(size_t)bh*SK*DH; const u16* V=vc+(size_t)bh*SK*DH;
  for(int i=threadIdx.x;i<SQ*SK;i+=blockDim.x){
    int qi=i/SK,ki=i%SK; float acc=0;
    for(int d=0;d<DH;++d) acc+=bf2f(q[(size_t)(b*SQ+qi)*HD+h*DH+d])*bf2f(K[ki*DH+d]);
    p[qi][ki]=acc*0.07216878364870322f;
  }
  __syncthreads();
  if(threadIdx.x<SQ){
    int qi=threadIdx.x; float m=-1e30f; for(int k=0;k<SK;++k) m=fmaxf(m,p[qi][k]);
    float s=0; for(int k=0;k<SK;++k){ float e=__expf(p[qi][k]-m); p[qi][k]=e; s+=e;}
    for(int k=0;k<SK;++k) p[qi][k]/=s;
  }
  __syncthreads();
  for(int i=threadIdx.x;i<SQ*DH;i+=blockDim.x){
    int qi=i/DH,d=i%DH; float acc=0;
    for(int k=0;k<SK;++k) acc+=p[qi][k]*bf2f(V[k*DH+d]);
    o[(size_t)(b*SQ+qi)*HD+h*DH+d]=f2bf(acc);
  }
}
__global__ void k_o(const u16* o,const u16* wo,u16* out){
  int n=blockIdx.x*128+threadIdx.x; int m=blockIdx.y;
  float acc=0;
  for(int k=0;k<HD;++k) acc+=bf2f(o[(size_t)m*HD+k])*bf2f(wo[(size_t)k*D+n]);
  out[(size_t)m*D+n]=f2bf(acc);
}


typedef unsigned v4u __attribute__((ext_vector_type(4)));
#define GAS __attribute__((address_space(1)))
struct Ptrs { const char* p[5]; long long n[5]; };
__global__ __launch_bounds__(512) void k_probe(const char* p0,const char* p1,const char* p2,const char* p3,const char* p4,unsigned long long* stamps,unsigned* sink){
  const int tid=threadIdx.x; const int bid=blockIdx.x;
  unsigned long long r0=__builtin_amdgcn_s_memrealtime();
  v4u acc={0,0,0,0};
  unsigned long long r[5];
  const char* ps[5]={p3,p0,p1,p2,p4};
  const int bytes[5]={7864320,983040,9437184,9437184,7864320};
  v4u v[20];
  int c=0;
  #pragma unroll
  for(int a=0;a<5;++a){
    int per=bytes[a]/256/16;
    const GAS v4u* q=(const GAS v4u*)(ps[a]+(size_t)bid*(bytes[a]/256));
    #pragma unroll
    for(int i=0;i<5;++i){ if(i*512<per){ int idx=i*512+tid; if(idx>=per) idx=per-1; v[c++]=q[idx]; } }
  }
  #pragma unroll
  for(int i=0;i<19;++i) if(i<c) acc^=v[i];
  unsigned long long r1=__builtin_amdgcn_s_memrealtime();
  if((acc.x^acc.y^acc.z^acc.w)==0x12345678) sink[tid]=1;
  __syncthreads();
  unsigned long long r2=__builtin_amdgcn_s_memrealtime();
  if(tid==0){ stamps[bid*2]=r0; stamps[bid*2+1]=r2; }
}
__global__ void k_report(unsigned long long* stamps){
  unsigned long long mn=~0ull,mx=0,mxs=0; double sum=0;
  for(int b=0;b<256;++b){ if(stamps[2*b]<mn) mn=stamps[2*b]; if(stamps[2*b]>mxs) mxs=stamps[2*b]; if(stamps[2*b+1]>mx) mx=stamps[2*b+1]; sum+=(double)(stamps[2*b+1]-stamps[2*b]); }
  printf("PROBE read36.6MB: wall %.2f us, avg per-block %.2f us, start spread %.2f us\n",(mx-mn)*0.01,sum/256*0.01,(mxs-mn)*0.01);
}

extern "C" void kernel_launch(void* const* d_in, const int* in_sizes, int n_in,
                              void* d_out, int out_size, void* d_ws, size_t ws_size,
                              hipStream_t stream) {
  const u16* x=(const u16*)d_in[0]; const u16* kc=(const u16*)d_in[1]; const u16* vc=(const u16*)d_in[2];
  const u16* wq=(const u16*)d_in[3]; const u16* wo=(const u16*)d_in[4];
  u16* q=(u16*)d_ws; u16* o=q+(size_t)B*SQ*HD;
  unsigned long long* st=(unsigned long long*)((char*)d_ws+(64<<20)); unsigned* sink=(unsigned*)((char*)d_ws+(65<<20));
  k_probe<<<256,512,0,stream>>>((const char*)x,(const char*)kc,(const char*)vc,(const char*)wq,(const char*)wo,st,sink);
  k_report<<<1,1,0,stream>>>(st);
  k_q<<<dim3(HD/256,B*SQ),256,0,stream>>>(x,wq,q);
  k_attn<<<B*H,256,0,stream>>>(q,kc,vc,o);
  k_o<<<dim3(D/128,B*SQ),128,0,stream>>>(o,wo,(u16*)d_out);
}
```

```cpp
#include <hip/hip_runtime.h>
#include <hip/hip_bf16.h>

typedef unsigned short u16;
__device__ __forceinline__ float bf2f(u16 b){ return __uint_as_float(((unsigned)b)<<16); }
__device__ __forceinline__ u16 f2bf(float f){ unsigned u=__float_as_uint(f); u += 0x7FFFu + ((u>>16)&1u); return (u16)(u>>16); }

constexpr int B=16,SQ=48,D=640,H=32,SK=48,DH=192,HD=H*DH;

__global__ void k_q(const u16* x,const u16* wq,u16* q){
  int n=blockIdx.x*256+threadIdx.x; int m=blockIdx.y;
  float acc=0;
  for(int k=0;k<D;++k) acc+=bf2f(x[m*D+k])*bf2f(wq[(size_t)k*HD+n]);
  q[(size_t)m*HD+n]=f2bf(acc);
}
__global__ void k_attn(const u16* q,const u16* kc,const u16* vc,u16* o){
  __shared__ float p[SQ][SK];
  int bh=blockIdx.x; int b=bh/H,h=bh%H;
  const u16* K=kc+(size_t)bh*SK*DH; const u16* V=vc+(size_t)bh*SK*DH;
  for(int i=threadIdx.x;i<SQ*SK;i+=blockDim.x){
    int qi=i/SK,ki=i%SK; float acc=0;
    for(int d=0;d<DH;++d) acc+=bf2f(q[(size_t)(b*SQ+qi)*HD+h*DH+d])*bf2f(K[ki*DH+d]);
    p[qi][ki]=acc*0.07216878364870322f;
  }
  __syncthreads();
  if(threadIdx.x<SQ){
    int qi=threadIdx.x; float m=-1e30f; for(int k=0;k<SK;++k) m=fmaxf(m,p[qi][k]);
    float s=0; for(int k=0;k<SK;++k){ float e=__expf(p[qi][k]-m); p[qi][k]=e; s+=e;}
    for(int k=0;k<SK;++k) p[qi][k]/=s;
  }
  __syncthreads();
  for(int i=threadIdx.x;i<SQ*DH;i+=blockDim.x){
    int qi=i/DH,d=i%DH; float acc=0;
    for(int k=0;k<SK;++k) acc+=p[qi][k]*bf2f(V[k*DH+d]);
    o[(size_t)(b*SQ+qi)*HD+h*DH+d]=f2bf(acc);
  }
}
__global__ void k_o(const u16* o,const u16* wo,u16* out){
  int n=blockIdx.x*128+threadIdx.x; int m=blockIdx.y;
  float acc=0;
  for(int k=0;k<HD;++k) acc+=bf2f(o[(size_t)m*HD+k])*bf2f(wo[(size_t)k*D+n]);
  out[(size_t)m*D+n]=f2bf(acc);
}


typedef unsigned v4u __attribute__((ext_vector_type(4)));
#define GAS __attribute__((address_space(1)))
struct Ptrs { const char* p[5]; long long n[5]; };
__global__ __launch_bounds__(512) void k_probe(const char* p0,const char* p1,const char* p2,const char* p3,const char* p4,unsigned long long* stamps,unsigned* sink){
  const int tid=threadIdx.x; const int bid=blockIdx.x;
  unsigned long long r0=__builtin_amdgcn_s_memrealtime();
  v4u acc={0,0,0,0};
  unsigned long long r[5];
  const char* ps[5]={p3,p0,p1,p2,p4};
  const int bytes[5]={7864320,983040,9437184,9437184,7864320};
  v4u v[20];
  int c=0;
  #pragma unroll
  for(int a=0;a<5;++a){
    int per=bytes[a]/256/16;
    const GAS v4u* q=(const GAS v4u*)(ps[a]+(size_t)bid*(bytes[a]/256));
    #pragma unroll
    for(int i=0;i<5;++i){ if(i*512<per){ int idx=i*512+tid; if(idx>=per) idx=per-1; v[c++]=q[idx]; } }
  }
  #pragma unroll
  for(int i=0;i<19;++i) if(i<c) acc^=v[i];
  unsigned long long r1=__builtin_amdgcn_s_memrealtime();
  if((acc.x^acc.y^acc.z^acc.w)==0x12345678) sink[tid]=1;
  __syncthreads();
  unsigned long long r2=__builtin_amdgcn_s_memrealtime();
  if(tid==0){ stamps[bid*2]=r0; stamps[bid*2+1]=r2; }
}
__global__ void k_report(unsigned long long* stamps,int mult){
  unsigned long long mn=~0ull,mx=0;
  for(int b=0;b<256;++b){ if(stamps[2*b]<mn) mn=stamps[2*b]; if(stamps[2*b+1]>mx) mx=stamps[2*b+1]; }
  unsigned long long d=(mx-mn); if(d>10000) d=10000;
  unsigned long long t0=__builtin_amdgcn_s_memrealtime();
  while(__builtin_amdgcn_s_memrealtime()-t0 < d*mult) __builtin_amdgcn_s_sleep(4);
}

extern "C" void kernel_launch(void* const* d_in, const int* in_sizes, int n_in,
                              void* d_out, int out_size, void* d_ws, size_t ws_size,
                              hipStream_t stream) {
  const u16* x=(const u16*)d_in[0]; const u16* kc=(const u16*)d_in[1]; const u16* vc=(const u16*)d_in[2];
  const u16* wq=(const u16*)d_in[3]; const u16* wo=(const u16*)d_in[4];
  u16* q=(u16*)d_ws; u16* o=q+(size_t)B*SQ*HD;
  unsigned long long* st=(unsigned long long*)((char*)d_ws+(64<<20)); unsigned* sink=(unsigned*)((char*)d_ws+(65<<20));
  k_probe<<<256,512,0,stream>>>((const char*)x,(const char*)kc,(const char*)vc,(const char*)wq,(const char*)wo,st,sink);
  k_report<<<1,1,0,stream>>>(st,0);
  k_q<<<dim3(HD/256,B*SQ),256,0,stream>>>(x,wq,q);
  k_attn<<<B*H,256,0,stream>>>(q,kc,vc,o);
  k_o<<<dim3(D/128,B*SQ),128,0,stream>>>(o,wo,(u16*)d_out);
}
```

```cpp
#include <hip/hip_runtime.h>
#include <hip/hip_bf16.h>

typedef unsigned short u16;
typedef unsigned long long u64;
typedef short bf16x8 __attribute__((ext_vector_type(8)));
typedef short s16x4 __attribute__((ext_vector_type(4)));
typedef float f32x4 __attribute__((ext_vector_type(4)));
typedef float f32x2 __attribute__((ext_vector_type(2)));
typedef __bf16 bf16x2_t __attribute__((ext_vector_type(2)));
typedef unsigned v4u __attribute__((ext_vector_type(4)));
typedef unsigned v2u __attribute__((ext_vector_type(2)));
#define GAS __attribute__((address_space(1)))
#define LAS __attribute__((address_space(3)))
#define CAS __attribute__((address_space(4)))
typedef GAS u64 gu64;
#define RLX_AGENT __ATOMIC_RELAXED, __HIP_MEMORY_SCOPE_AGENT

constexpr int NB = 16, SQ = 48, DM = 640, NH = 32, SK = 48, DH = 192, HD = NH * DH;
constexpr int SLOT = 36864;
constexpr int LDS_BYTES = 4 * SLOT;
constexpr int X_OFF = 24576;
constexpr int V_OFF = 20480;
constexpr int WS_EPOCH = 0;
constexpr int WS_FLAGP = 4096;
constexpr int WS_FLAGO = WS_FLAGP + 256 * 128;
constexpr int WS_P32 = 1 << 17;
constexpr int WS_P16 = WS_P32 + 256 * 6144;
constexpr int WS_PART = WS_P16 + 256 * 3072;
constexpr int WS_END = WS_PART + 256 * 32768;
constexpr unsigned SPIN_CAP = 1u << 22;

#define SBAR() __builtin_amdgcn_sched_barrier(0)
#define BARRIER() asm volatile("s_barrier" ::: "memory")
#define LGKM0_BARRIER() asm volatile("s_waitcnt lgkmcnt(0)\n\ts_barrier" ::: "memory")

__device__ __forceinline__ f32x4 mfma(bf16x8 a, bf16x8 b, f32x4 c) { return __builtin_amdgcn_mfma_f32_16x16x32_bf16(a, b, c, 0, 0, 0); }
__device__ __forceinline__ unsigned pk2(float lo, float hi) {
    f32x2 v = {lo, hi};
    return __builtin_bit_cast(unsigned, __builtin_convertvector(v, bf16x2_t));
}
__device__ __forceinline__ v2u pk4(f32x4 v) { return v2u{pk2(v[0], v[1]), pk2(v[2], v[3])}; }
__device__ __forceinline__ bf16x8 cat(v2u lo, v2u hi) { v4u w = {lo.x, lo.y, hi.x, hi.y}; return __builtin_bit_cast(bf16x8, w); }
typedef short v4i16_t __attribute__((ext_vector_type(4)));
__device__ __forceinline__ v2u tr_read(LAS const char* p) {
    return __builtin_bit_cast(v2u, __builtin_amdgcn_ds_read_tr16_b64_v4i16((LAS v4i16_t*)p));
}
__device__ __forceinline__ bf16x8 ld128(LAS const char* p) { return *(LAS const bf16x8*)p; }
__device__ __forceinline__ void dma16(const char* sbase, unsigned voff, unsigned lds) {
    asm volatile("s_mov_b32 m0, %0\n\ts_nop 0\n\tglobal_load_lds_dwordx4 %1, %2" ::"s"(lds), "v"(voff), "s"(sbase) : "memory");
}
__device__ __forceinline__ float xrow_max(float x) {
    auto s = __builtin_amdgcn_permlane16_swap(__float_as_uint(x), __float_as_uint(x), false, false);
    x = __builtin_fmaxf(__uint_as_float(s[0]), __uint_as_float(s[1]));
    auto t = __builtin_amdgcn_permlane32_swap(__float_as_uint(x), __float_as_uint(x), false, false);
    return __builtin_fmaxf(__uint_as_float(t[0]), __uint_as_float(t[1]));
}
__device__ __forceinline__ float xrow_sum(float x) {
    auto s = __builtin_amdgcn_permlane16_swap(__float_as_uint(x), __float_as_uint(x), false, false);
    x = __uint_as_float(s[0]) + __uint_as_float(s[1]);
    auto t = __builtin_amdgcn_permlane32_swap(__float_as_uint(x), __float_as_uint(x), false, false);
    return __uint_as_float(t[0]) + __uint_as_float(t[1]);
}
__device__ __forceinline__ void poll_flags(gu64* base, int stride_words, int n, u64 tag, int lane) {
    for (unsigned spins = 0; spins < SPIN_CAP; ++spins) {
        u64 v = tag;
        if (lane < n) v = __hip_atomic_load(base + lane * stride_words, RLX_AGENT);
        if (__all(v == tag)) break;
        __builtin_amdgcn_s_sleep(1);
    }
    __builtin_amdgcn_fence(__ATOMIC_ACQUIRE, "wavefront");
}

__global__ __launch_bounds__(512, 2) void mha_fused(const u16* __restrict__ x, const u16* __restrict__ kc, const u16* __restrict__ vc,
                                                    const u16* __restrict__ wq, const u16* __restrict__ wo, u16* __restrict__ out, char* __restrict__ ws) {
    extern __shared__ __attribute__((aligned(16))) char smem[];
    const int tid = threadIdx.x, lane = tid & 63;
    const int w = __builtin_amdgcn_readfirstlane(tid >> 6);
    const int bid = blockIdx.x, s = bid & 7, p = bid >> 5, j = (bid >> 3) & 3, h = 4 * s + j;
    const u64 tag = *(CAS const u64*)(ws + WS_EPOCH + p * 128) + 1ull;
    asm volatile("" ::"s"(tag));
    const unsigned lds0 = (unsigned)(uintptr_t)(LAS char*)smem;
    const int rot1 = (p + 3 * j) % 10;
    const int hrot = (p + j) & 3;
    const int drot = (p >> 2);

    if (w >= 4) {
        const int lw = w - 4;
        const int r8 = lane >> 3;
        const unsigned fx = ((lw & 1) << 2) | (r8 >> 1);
        const unsigned voff_wq = r8 * 12288 + (((lane & 7) ^ ((((r8 >> 1) & 1) << 1) | ((lw & 1) << 2))) << 4);
        const unsigned voff_x = r8 * 1280 + (((lane & 7) ^ fx) << 4);
        const unsigned voff_v = r8 * 384 + (((lane & 7) ^ fx) << 4);
        const unsigned voff_wo = (lane >> 2) * 1280 + (((lane & 3) ^ (((lane >> 5) & 1) << 1)) << 4);
        const char* wq_b = (const char*)wq + (size_t)lw * 8 * 12288 + h * 384;
        const char* x_b = (const char*)x + (size_t)(96 * p + 8 * lw) * 1280;
        const char* wo_b = (const char*)wo + (size_t)lw * 16 * 1280 + j * 320;
        auto issue1 = [&](int step) {
            int kk = step + rot1; if (kk >= 10) kk -= 10;
            const unsigned slot = lds0 + (step & 3) * SLOT;
            const char* a = wq_b + (size_t)kk * 64 * 12288;
#pragma unroll
            for (int i = 0; i < 2; ++i)
#pragma unroll
                for (int cb = 0; cb < 3; ++cb) dma16(a + i * 32 * 12288 + cb * 128, voff_wq, slot + ((lw + 4 * i) * 3 + cb) * 1024);
            const char* b = x_b + kk * 128;
#pragma unroll
            for (int i = 0; i < 3; ++i) dma16(b + i * 32 * 1280, voff_x, slot + X_OFF + (lw + 4 * i) * 1024);
        };
        auto issue2 = [&](int step) {
            const int t = step - 11, hh = t / 3;
            int sub = t - 3 * hh + drot; if (sub >= 3) sub -= 3;
            const int hp = 4 * s + ((hh + hrot) & 3);
            const unsigned slot = lds0 + (step & 3) * SLOT;
            const char* a = wo_b + (size_t)(hp * 192 + sub * 64) * 1280;
#pragma unroll
            for (int cb = 0; cb < 5; ++cb) dma16(a + cb * 64, voff_wo, slot + (lw * 5 + cb) * 1024);
#pragma unroll
            for (int i = 0; i < 3; ++i) {
                const int pi = lw + 4 * i, bi = pi >= 6 ? 1 : 0, kr = pi - 6 * bi;
                dma16((const char*)vc + (size_t)(((2 * p + bi) * 32 + hp) * 48 + 8 * kr) * 384 + sub * 128, voff_v, slot + V_OFF + pi * 1024);
            }
        };
        issue1(0); issue1(1); issue1(2);
        asm volatile("s_waitcnt vmcnt(9)\n\ts_barrier" ::: "memory");
        for (int k = 0; k <= 21; ++k) {
            const int n = k + 3;
            if (n <= 9) { issue1(n); asm volatile("s_waitcnt vmcnt(9)" ::: "memory"); }
            else if (n >= 11 && n <= 22) { issue2(n); asm volatile("s_waitcnt vmcnt(8)" ::: "memory"); }
            else asm volatile("s_waitcnt vmcnt(0)" ::: "memory");
            if (k == 10) BARRIER();
            if (k == 20 && w == 4) poll_flags((gu64*)(ws + WS_FLAGP) + (size_t)(p * 32 + 4 * s) * 16, 16, 4, tag, lane);
            BARRIER();
        }
        BARRIER();
        if (w == 4) {
            if (lane == 0) __hip_atomic_store((gu64*)(ws + WS_FLAGO) + (size_t)((p * 4 + j) * 8 + s) * 16, tag, RLX_AGENT);
            poll_flags((gu64*)(ws + WS_FLAGO) + (size_t)((p * 4 + j) * 8) * 16, 16, 8, tag, lane);
            if (s == 0 && j == 0 && lane == 0) __hip_atomic_store((gu64*)(ws + WS_EPOCH + p * 128), tag, RLX_AGENT);
        }
        BARRIER();
        return;
    }

    const int bi = w >> 1, hf = w & 1;
    const int g = lane >> 4, l15 = lane & 15, q4 = (lane >> 2) & 3, p4 = lane & 3;
    const __amdgpu_buffer_rsrc_t wsr = __builtin_amdgcn_make_buffer_rsrc(ws, 0, WS_END, 0x00020000);
    LAS const char* L = (LAS const char*)smem;

    v2u kf[3][3][2];
    {
        const char* kb = (const char*)kc + (size_t)(((2 * p + bi) * 32 + h) * 48 + l15) * 384 + hf * 192 + g * 8;
#pragma unroll
        for (int kt = 0; kt < 3; ++kt)
#pragma unroll
            for (int u = 0; u < 3; ++u) {
                kf[kt][u][0] = *(GAS const v2u*)(kb + kt * 16 * 384 + u * 64);
                kf[kt][u][1] = *(GAS const v2u*)(kb + kt * 16 * 384 + u * 64 + 32);
            }
    }
    f32x4 acc[6][3];
#pragma unroll
    for (int t = 0; t < 6; ++t)
#pragma unroll
        for (int q = 0; q < 3; ++q) acc[t][q] = f32x4{0.f, 0.f, 0.f, 0.f};
    int aoff[6];
    {
        const int f = (((q4 >> 1) & 1) << 1) | ((g & 1) << 2);
#pragma unroll
        for (int t = 0; t < 6; ++t) {
            const int T = 6 * hf + t;
            aoff[t] = g * 3072 + q4 * 128 + (((2 * (T & 3) + (p4 >> 1)) ^ f) << 4) + (p4 & 1) * 8 + (T >> 2) * 1024;
        }
    }
    int boff[2];
#pragma unroll
    for (int ss = 0; ss < 2; ++ss) boff[ss] = (bi * 48 + l15) * 128 + (((4 * ss + g) ^ ((l15 >> 1) & 7)) << 4);

    bf16x8 fa[2][6], fb[2][3];
    auto read1 = [&](int slot, int ss, bf16x8* A, bf16x8* B) {
#pragma unroll
        for (int q = 0; q < 3; ++q) B[q] = ld128(L + slot + X_OFF + boff[ss] + q * 2048);
#pragma unroll
        for (int t = 0; t < 6; ++t) A[t] = cat(tr_read(L + slot + aoff[t] + ss * 12288), tr_read(L + slot + aoff[t] + ss * 12288 + 512));
    };
    BARRIER();
    read1(0, 0, fa[0], fb[0]);
    for (int k = 0; k < 10; ++k) {
        const int slot = (k & 3) * SLOT, nslot = ((k + 1) & 3) * SLOT;
        read1(slot, 1, fa[1], fb[1]);
#pragma unroll
        for (int t = 0; t < 6; ++t)
#pragma unroll
            for (int q = 0; q < 3; ++q) acc[t][q] = mfma(fa[0][t], fb[0][q], acc[t][q]);
        if (k < 9) read1(nslot, 0, fa[0], fb[0]);
#pragma unroll
        for (int t = 0; t < 6; ++t)
#pragma unroll
            for (int q = 0; q < 3; ++q) acc[t][q] = mfma(fa[1][t], fb[1][q], acc[t][q]);
        SBAR();
        LGKM0_BARRIER();
    }
    {
        f32x4 sp[3][3];
#pragma unroll
        for (int kt = 0; kt < 3; ++kt)
#pragma unroll
            for (int q = 0; q < 3; ++q) sp[kt][q] = f32x4{0.f, 0.f, 0.f, 0.f};
#pragma unroll
        for (int u = 0; u < 3; ++u)
#pragma unroll
            for (int q = 0; q < 3; ++q) {
                const bf16x8 qf = cat(pk4(acc[2 * u][q]), pk4(acc[2 * u + 1][q]));
#pragma unroll
                for (int kt = 0; kt < 3; ++kt) sp[kt][q] = mfma(cat(kf[kt][u][0], kf[kt][u][1]), qf, sp[kt][q]);
            }
        LAS char* X = (LAS char*)smem + 2 * SLOT;
#pragma unroll
        for (int kt = 0; kt < 3; ++kt)
#pragma unroll
            for (int q = 0; q < 3; ++q) *(LAS f32x4*)(X + w * 9216 + (kt * 3 + q) * 1024 + lane * 16) = sp[kt][q];
        LGKM0_BARRIER();
#pragma unroll
        for (int kt = 0; kt < 3; ++kt)
#pragma unroll
            for (int q = 0; q < 3; ++q) sp[kt][q] += *(LAS const f32x4*)(X + (w ^ 1) * 9216 + (kt * 3 + q) * 1024 + lane * 16);
        constexpr float C2 = 0.07216878364870322f * 1.4426950408889634f;
        const int pbase = (bid * 2 + bi) * 3;
#pragma unroll
        for (int q = 0; q < 3; ++q) {
            float m = __builtin_fmaxf(__builtin_fmaxf(sp[0][q][0], sp[0][q][1]), __builtin_fmaxf(sp[0][q][2], sp[0][q][3]));
#pragma unroll
            for (int kt = 1; kt < 3; ++kt)
                m = __builtin_fmaxf(m, __builtin_fmaxf(__builtin_fmaxf(sp[kt][q][0], sp[kt][q][1]), __builtin_fmaxf(sp[kt][q][2], sp[kt][q][3])));
            m = xrow_max(m);
            const float nm = -m * C2;
            float sum = 0.f;
#pragma unroll
            for (int kt = 0; kt < 3; ++kt)
#pragma unroll
                for (int r = 0; r < 4; ++r) { const float e = __builtin_amdgcn_exp2f(__builtin_fmaf(sp[kt][q][r], C2, nm)); sp[kt][q][r] = e; sum += e; }
            sum = xrow_sum(sum);
            const float inv = 1.0f / sum;
#pragma unroll
            for (int kt = 0; kt < 3; ++kt) sp[kt][q] *= inv;
            if (hf == 0) {
                const v2u a = pk4(sp[0][q]), b = pk4(sp[1][q]);
                __builtin_amdgcn_raw_buffer_store_b128(v4u{a.x, a.y, b.x, b.y}, wsr, WS_P32 + (pbase + q) * 1024 + lane * 16, 0, 16);
            } else {
                __builtin_amdgcn_raw_buffer_store_b64(pk4(sp[2][q]), wsr, WS_P16 + (pbase + q) * 512 + lane * 8, 0, 16);
            }
        }
    }
    int woff[5];
#pragma unroll
    for (int nt = 0; nt < 5; ++nt) {
        const int NT = 5 * hf + nt;
        woff[nt] = (g >> 1) * 5120 + (NT >> 1) * 1024 + ((g & 1) * 8 + q4) * 64 + (((2 * (NT & 1) + (p4 >> 1)) ^ ((g & 1) << 1)) << 4) + (p4 & 1) * 8;
    }
    bf16x8 va[2][3], wb[2][5];
    auto read2 = [&](int slot, int ss, bf16x8* A, bf16x8* B) {
#pragma unroll
        for (int kt = 0; kt < 3; ++kt) A[kt] = ld128(L + slot + V_OFF + boff[ss] + kt * 2048);
#pragma unroll
        for (int nt = 0; nt < 5; ++nt) B[nt] = cat(tr_read(L + slot + woff[nt] + ss * 10240), tr_read(L + slot + woff[nt] + ss * 10240 + 256));
    };
    read2((11 & 3) * SLOT, 0, va[0], wb[0]);
    SBAR();
    LGKM0_BARRIER();
    v2u vw[4][3][5];
    v4u p32[4][3]; v2u p16[4][3];
#pragma unroll
    for (int hh = 0; hh < 4; ++hh) {
        f32x4 a2[3][5];
#pragma unroll
        for (int kt = 0; kt < 3; ++kt)
#pragma unroll
            for (int nt = 0; nt < 5; ++nt) a2[kt][nt] = f32x4{0.f, 0.f, 0.f, 0.f};
#pragma unroll
        for (int sub = 0; sub < 3; ++sub) {
            const int k = 11 + 3 * hh + sub;
            const int slot = (k & 3) * SLOT, nslot = ((k + 1) & 3) * SLOT;
            if (k == 14 && tid == 0) __hip_atomic_store((gu64*)(ws + WS_FLAGP) + (size_t)(p * 32 + h) * 16, tag, RLX_AGENT);
            read2(slot, 1, va[1], wb[1]);
#pragma unroll
            for (int kt = 0; kt < 3; ++kt)
#pragma unroll
                for (int nt = 0; nt < 5; ++nt) a2[kt][nt] = mfma(va[0][kt], wb[0][nt], a2[kt][nt]);
            if (k < 22) read2(nslot, 0, va[0], wb[0]);
#pragma unroll
            for (int kt = 0; kt < 3; ++kt)
#pragma unroll
                for (int nt = 0; nt < 5; ++nt) a2[kt][nt] = mfma(va[1][kt], wb[1][nt], a2[kt][nt]);
            if (k == 13) asm volatile("s_waitcnt vmcnt(0)" ::: "memory");
            if (k < 22) { SBAR(); LGKM0_BARRIER(); }
        }
#pragma unroll
        for (int kt = 0; kt < 3; ++kt)
#pragma unroll
            for (int nt = 0; nt < 5; ++nt) vw[hh][kt][nt] = pk4(a2[kt][nt]);
    }
    {
        v2u hold = {0u, 0u};
        const int obase = WS_PART + (bid * 4 + w) * 8192 + lane * 16;
#pragma unroll
        for (int q = 0; q < 3; ++q) {
#pragma unroll
            for (int hh = 0; hh < 4; ++hh) {
                const int src = (p * 32 + 4 * s + ((hh + hrot) & 3));
                const int sb = (((src >> 5) * 32 + ((src & 3) * 8) + ((src >> 2) & 7)) * 2 + bi) * 3 + q;
                p32[hh][q] = __builtin_amdgcn_raw_buffer_load_b128(wsr, WS_P32 + sb * 1024 + lane * 16, 0, 16);
                p16[hh][q] = __builtin_amdgcn_raw_buffer_load_b64(wsr, WS_P16 + sb * 512 + lane * 8, 0, 16);
            }
        }
#pragma unroll
        for (int q = 0; q < 3; ++q) {
            f32x4 o[5];
#pragma unroll
            for (int nt = 0; nt < 5; ++nt) o[nt] = f32x4{0.f, 0.f, 0.f, 0.f};
#pragma unroll
            for (int hh = 0; hh < 4; ++hh)
#pragma unroll
                for (int nt = 0; nt < 5; ++nt) o[nt] = mfma(cat(vw[hh][0][nt], vw[hh][1][nt]), __builtin_bit_cast(bf16x8, p32[hh][q]), o[nt]);
#pragma unroll
            for (int hp = 0; hp < 2; ++hp)
#pragma unroll
                for (int nt = 0; nt < 5; ++nt) o[nt] = mfma(cat(vw[2 * hp][2][nt], vw[2 * hp + 1][2][nt]), cat(p16[2 * hp][q], p16[2 * hp + 1][q]), o[nt]);
#pragma unroll
            for (int nt = 0; nt < 5; ++nt) {
                const int t = 5 * q + nt;
                const v2u v = pk4(o[nt]);
                if (t == 14) __builtin_amdgcn_raw_buffer_store_b128(v4u{v.x, v.y, 0u, 0u}, wsr, obase + 7 * 1024, 0, 16);
                else if (t & 1) __builtin_amdgcn_raw_buffer_store_b128(v4u{hold.x, hold.y, v.x, v.y}, wsr, obase + (t >> 1) * 1024, 0, 16);
                else hold = v;
            }
        }
    }
    asm volatile("s_waitcnt vmcnt(0)" ::: "memory");
    BARRIER();
    BARRIER();
    {
        v4u a[8];
#pragma unroll
        for (int m = 0; m < 8; ++m) {
            const int sb = p * 32 + j * 8 + m;
            a[m] = __builtin_amdgcn_raw_buffer_load_b128(wsr, WS_PART + (sb * 4 + w) * 8192 + s * 1024 + lane * 16, 0, 16);
        }
        float r[8];
#pragma unroll
        for (int i = 0; i < 8; ++i) r[i] = 0.f;
#pragma unroll
        for (int m = 0; m < 8; ++m)
#pragma unroll
            for (int i = 0; i < 4; ++i) { r[2 * i] += __uint_as_float(a[m][i] << 16); r[2 * i + 1] += __uint_as_float(a[m][i] & 0xffff0000u); }
        const int t0 = 2 * s, t1 = 2 * s + 1;
        {
            const int q = t0 / 5, nt = t0 - 5 * q;
            u16* o = out + (size_t)((2 * p + bi) * 48 + 16 * q + l15) * DM + 160 * j + 80 * hf + 16 * nt + 4 * g;
            *(GAS v2u*)o = v2u{pk2(r[0], r[1]), pk2(r[2], r[3])};
        }
        if (t1 < 15) {
            const int q = t1 / 5, nt = t1 - 5 * q;
            u16* o = out + (size_t)((2 * p + bi) * 48 + 16 * q + l15) * DM + 160 * j + 80 * hf + 16 * nt + 4 * g;
            *(GAS v2u*)o = v2u{pk2(r[4], r[5]), pk2(r[6], r[7])};
        }
    }
}

extern "C" void kernel_launch(void* const* d_in, const int* in_sizes, int n_in, void* d_out, int out_size, void* d_ws, size_t ws_size,
                              hipStream_t stream) {
    (void)in_sizes; (void)n_in; (void)out_size; (void)ws_size;
    hipFuncSetAttribute((const void*)mha_fused, hipFuncAttributeMaxDynamicSharedMemorySize, LDS_BYTES);
    mha_fused<<<256, 512, LDS_BYTES, stream>>>((const u16*)d_in[0], (const u16*)d_in[1], (const u16*)d_in[2], (const u16*)d_in[3],
                                               (const u16*)d_in[4], (u16*)d_out, (char*)d_ws);
}
```

```cpp
#define AMP_W 0
#define AMP_IDX 0
#define AMP_AVG 1
#include <hip/hip_runtime.h>
#include <hip/hip_bf16.h>

typedef unsigned short u16;
typedef unsigned long long u64;
typedef short bf16x8 __attribute__((ext_vector_type(8)));
typedef short s16x4 __attribute__((ext_vector_type(4)));
typedef float f32x4 __attribute__((ext_vector_type(4)));
typedef float f32x2 __attribute__((ext_vector_type(2)));
typedef __bf16 bf16x2_t __attribute__((ext_vector_type(2)));
typedef unsigned v4u __attribute__((ext_vector_type(4)));
typedef unsigned v2u __attribute__((ext_vector_type(2)));
#define GAS __attribute__((address_space(1)))
#define LAS __attribute__((address_space(3)))
#define CAS __attribute__((address_space(4)))
typedef GAS u64 gu64;
#define RLX_AGENT __ATOMIC_RELAXED, __HIP_MEMORY_SCOPE_AGENT

constexpr int NB = 16, SQ = 48, DM = 640, NH = 32, SK = 48, DH = 192, HD = NH * DH;
constexpr int SLOT = 36864;
constexpr int LDS_BYTES = 4 * SLOT;
constexpr int X_OFF = 24576;
constexpr int V_OFF = 20480;
constexpr int WS_EPOCH = 0;
constexpr int WS_FLAGP = 4096;
constexpr int WS_FLAGO = WS_FLAGP + 256 * 128;
constexpr int WS_P32 = 1 << 17;
constexpr int WS_P16 = WS_P32 + 256 * 6144;
constexpr int WS_PART = WS_P16 + 256 * 3072;
constexpr int WS_END = WS_PART + 256 * 32768;
constexpr unsigned SPIN_CAP = 1u << 22;

#define NST 12
#define STAMP(i) do{ st[i]=__builtin_amdgcn_s_memrealtime(); }while(0)
#define STAMP_DECL u64 st[NST]; _Pragma("unroll") for(int i_=0;i_<NST;++i_) st[i_]=0
#define STAMP_OUT(role) do{ if(lane==0){ _Pragma("unroll") for(int i_=0;i_<NST;++i_) ((u64*)(ws+WS_END))[(size_t)(bid*8+w)*NST+i_]=st[i_]; } }while(0)
#define SBAR() __builtin_amdgcn_sched_barrier(0)
#define BARRIER() asm volatile("s_barrier" ::: "memory")
#define LGKM0_BARRIER() asm volatile("s_waitcnt lgkmcnt(0)\n\ts_barrier" ::: "memory")

__device__ __forceinline__ f32x4 mfma(bf16x8 a, bf16x8 b, f32x4 c) { return __builtin_amdgcn_mfma_f32_16x16x32_bf16(a, b, c, 0, 0, 0); }
__device__ __forceinline__ unsigned pk2(float lo, float hi) {
    f32x2 v = {lo, hi};
    return __builtin_bit_cast(unsigned, __builtin_convertvector(v, bf16x2_t));
}
__device__ __forceinline__ v2u pk4(f32x4 v) { return v2u{pk2(v[0], v[1]), pk2(v[2], v[3])}; }
__device__ __forceinline__ bf16x8 cat(v2u lo, v2u hi) { v4u w = {lo.x, lo.y, hi.x, hi.y}; return __builtin_bit_cast(bf16x8, w); }
typedef short v4i16_t __attribute__((ext_vector_type(4)));
__device__ __forceinline__ v2u tr_read(LAS const char* p) {
    return __builtin_bit_cast(v2u, __builtin_amdgcn_ds_read_tr16_b64_v4i16((LAS v4i16_t*)p));
}
__device__ __forceinline__ bf16x8 ld128(LAS const char* p) { return *(LAS const bf16x8*)p; }
__device__ __forceinline__ void dma16(const char* sbase, unsigned voff, unsigned lds) {
    asm volatile("s_mov_b32 m0, %0\n\ts_nop 0\n\tglobal_load_lds_dwordx4 %1, %2" ::"s"(lds), "v"(voff), "s"(sbase) : "memory");
}
__device__ __forceinline__ float xrow_max(float x) {
    auto s = __builtin_amdgcn_permlane16_swap(__float_as_uint(x), __float_as_uint(x), false, false);
    x = __builtin_fmaxf(__uint_as_float(s[0]), __uint_as_float(s[1]));
    auto t = __builtin_amdgcn_permlane32_swap(__float_as_uint(x), __float_as_uint(x), false, false);
    return __builtin_fmaxf(__uint_as_float(t[0]), __uint_as_float(t[1]));
}
__device__ __forceinline__ float xrow_sum(float x) {
    auto s = __builtin_amdgcn_permlane16_swap(__float_as_uint(x), __float_as_uint(x), false, false);
    x = __uint_as_float(s[0]) + __uint_as_float(s[1]);
    auto t = __builtin_amdgcn_permlane32_swap(__float_as_uint(x), __float_as_uint(x), false, false);
    return __uint_as_float(t[0]) + __uint_as_float(t[1]);
}
__device__ __forceinline__ void poll_flags(gu64* base, int stride_words, int n, u64 tag, int lane) {
    for (unsigned spins = 0; spins < SPIN_CAP; ++spins) {
        u64 v = tag;
        if (lane < n) v = __hip_atomic_load(base + lane * stride_words, RLX_AGENT);
        if (__all(v == tag)) break;
        __builtin_amdgcn_s_sleep(1);
    }
    __builtin_amdgcn_fence(__ATOMIC_ACQUIRE, "wavefront");
}

__global__ __launch_bounds__(512, 2) void mha_fused(const u16* __restrict__ x, const u16* __restrict__ kc, const u16* __restrict__ vc,
                                                    const u16* __restrict__ wq, const u16* __restrict__ wo, u16* __restrict__ out, char* __restrict__ ws) {
    extern __shared__ __attribute__((aligned(16))) char smem[];
    const int tid = threadIdx.x, lane = tid & 63;
    const int w = __builtin_amdgcn_readfirstlane(tid >> 6);
    STAMP_DECL; STAMP(0);
    const int bid = blockIdx.x, s = bid & 7, p = bid >> 5, j = (bid >> 3) & 3, h = 4 * s + j;
    const u64 tag = *(CAS const u64*)(ws + WS_EPOCH + p * 128) + 1ull;
    asm volatile("" ::"s"(tag));
    const unsigned lds0 = (unsigned)(uintptr_t)(LAS char*)smem;
    const int rot1 = (p + 3 * j) % 10;
    const int hrot = (p + j) & 3;
    const int drot = (p >> 2);

    if (w >= 4) {
        const int lw = w - 4;
        const int r8 = lane >> 3;
        const unsigned fx = ((lw & 1) << 2) | (r8 >> 1);
        const unsigned voff_wq = r8 * 12288 + (((lane & 7) ^ ((((r8 >> 1) & 1) << 1) | ((lw & 1) << 2))) << 4);
        const unsigned voff_x = r8 * 1280 + (((lane & 7) ^ fx) << 4);
        const unsigned voff_v = r8 * 384 + (((lane & 7) ^ fx) << 4);
        const unsigned voff_wo = (lane >> 2) * 1280 + (((lane & 3) ^ (((lane >> 5) & 1) << 1)) << 4);
        const char* wq_b = (const char*)wq + (size_t)lw * 8 * 12288 + h * 384;
        const char* x_b = (const char*)x + (size_t)(96 * p + 8 * lw) * 1280;
        const char* wo_b = (const char*)wo + (size_t)lw * 16 * 1280 + j * 320;
        auto issue1 = [&](int step) {
            int kk = step + rot1; if (kk >= 10) kk -= 10;
            const unsigned slot = lds0 + (step & 3) * SLOT;
            const char* a = wq_b + (size_t)kk * 64 * 12288;
#pragma unroll
            for (int i = 0; i < 2; ++i)
#pragma unroll
                for (int cb = 0; cb < 3; ++cb) dma16(a + i * 32 * 12288 + cb * 128, voff_wq, slot + ((lw + 4 * i) * 3 + cb) * 1024);
            const char* b = x_b + kk * 128;
#pragma unroll
            for (int i = 0; i < 3; ++i) dma16(b + i * 32 * 1280, voff_x, slot + X_OFF + (lw + 4 * i) * 1024);
        };
        auto issue2 = [&](int step) {
            const int t = step - 11, hh = t / 3;
            int sub = t - 3 * hh + drot; if (sub >= 3) sub -= 3;
            const int hp = 4 * s + ((hh + hrot) & 3);
            const unsigned slot = lds0 + (step & 3) * SLOT;
            const char* a = wo_b + (size_t)(hp * 192 + sub * 64) * 1280;
#pragma unroll
            for (int cb = 0; cb < 5; ++cb) dma16(a + cb * 64, voff_wo, slot + (lw * 5 + cb) * 1024);
#pragma unroll
            for (int i = 0; i < 3; ++i) {
                const int pi = lw + 4 * i, bi = pi >= 6 ? 1 : 0, kr = pi - 6 * bi;
                dma16((const char*)vc + (size_t)(((2 * p + bi) * 32 + hp) * 48 + 8 * kr) * 384 + sub * 128, voff_v, slot + V_OFF + pi * 1024);
            }
        };
        asm volatile("" ::"s"(wq_b), "s"(x_b), "s"(wo_b), "v"(voff_wq));
        STAMP(10);
        issue1(0);
        STAMP(11);
        issue1(1); issue1(2);
        STAMP(1);
        asm volatile("s_waitcnt vmcnt(9)" ::: "memory"); STAMP(2);
        BARRIER();
        for (int k = 0; k <= 21; ++k) {
            const int n = k + 3;
            if (n <= 9) { issue1(n); asm volatile("s_waitcnt vmcnt(9)" ::: "memory"); }
            else if (n >= 11 && n <= 22) { issue2(n); asm volatile("s_waitcnt vmcnt(8)" ::: "memory"); }
            else asm volatile("s_waitcnt vmcnt(0)" ::: "memory");
            if (k == 10) BARRIER();
            if (k == 20 && w == 4) poll_flags((gu64*)(ws + WS_FLAGP) + (size_t)(p * 32 + 4 * s) * 16, 16, 4, tag, lane);
            BARRIER();
            if (k == 9) STAMP(3);
            if (k == 10) STAMP(4);
            if (k == 20) STAMP(5);
        }
        STAMP(6);
        BARRIER();
        STAMP(7);
        if (w == 4) {
            if (lane == 0) __hip_atomic_store((gu64*)(ws + WS_FLAGO) + (size_t)((p * 4 + j) * 8 + s) * 16, tag, RLX_AGENT);
            poll_flags((gu64*)(ws + WS_FLAGO) + (size_t)((p * 4 + j) * 8) * 16, 16, 8, tag, lane);
            if (s == 0 && j == 0 && lane == 0) __hip_atomic_store((gu64*)(ws + WS_EPOCH + p * 128), tag, RLX_AGENT);
        }
        STAMP(8);
        BARRIER();
        STAMP(9);
        STAMP_OUT(1);
        return;
    }

    const int bi = w >> 1, hf = w & 1;
    const int g = lane >> 4, l15 = lane & 15, q4 = (lane >> 2) & 3, p4 = lane & 3;
    const __amdgpu_buffer_rsrc_t wsr = __builtin_amdgcn_make_buffer_rsrc(ws, 0, WS_END, 0x00020000);
    LAS const char* L = (LAS const char*)smem;

    v2u kf[3][3][2];
    {
        const char* kb = (const char*)kc + (size_t)(((2 * p + bi) * 32 + h) * 48 + l15) * 384 + hf * 192 + g * 8;
#pragma unroll
        for (int kt = 0; kt < 3; ++kt)
#pragma unroll
            for (int u = 0; u < 3; ++u) {
                kf[kt][u][0] = *(GAS const v2u*)(kb + kt * 16 * 384 + u * 64);
                kf[kt][u][1] = *(GAS const v2u*)(kb + kt * 16 * 384 + u * 64 + 32);
            }
    }
    f32x4 acc[6][3];
#pragma unroll
    for (int t = 0; t < 6; ++t)
#pragma unroll
        for (int q = 0; q < 3; ++q) acc[t][q] = f32x4{0.f, 0.f, 0.f, 0.f};
    int aoff[6];
    {
        const int f = (((q4 >> 1) & 1) << 1) | ((g & 1) << 2);
#pragma unroll
        for (int t = 0; t < 6; ++t) {
            const int T = 6 * hf + t;
            aoff[t] = g * 3072 + q4 * 128 + (((2 * (T & 3) + (p4 >> 1)) ^ f) << 4) + (p4 & 1) * 8 + (T >> 2) * 1024;
        }
    }
    int boff[2];
#pragma unroll
    for (int ss = 0; ss < 2; ++ss) boff[ss] = (bi * 48 + l15) * 128 + (((4 * ss + g) ^ ((l15 >> 1) & 7)) << 4);

    bf16x8 fa[2][6], fb[2][3];
    auto read1 = [&](int slot, int ss, bf16x8* A, bf16x8* B) {
#pragma unroll
        for (int q = 0; q < 3; ++q) B[q] = ld128(L + slot + X_OFF + boff[ss] + q * 2048);
#pragma unroll
        for (int t = 0; t < 6; ++t) A[t] = cat(tr_read(L + slot + aoff[t] + ss * 12288), tr_read(L + slot + aoff[t] + ss * 12288 + 512));
    };
    STAMP(1);
    BARRIER();
    STAMP(2);
    read1(0, 0, fa[0], fb[0]);
    for (int k = 0; k < 10; ++k) {
        const int slot = (k & 3) * SLOT, nslot = ((k + 1) & 3) * SLOT;
        read1(slot, 1, fa[1], fb[1]);
#pragma unroll
        for (int t = 0; t < 6; ++t)
#pragma unroll
            for (int q = 0; q < 3; ++q) acc[t][q] = mfma(fa[0][t], fb[0][q], acc[t][q]);
        if (k < 9) read1(nslot, 0, fa[0], fb[0]);
#pragma unroll
        for (int t = 0; t < 6; ++t)
#pragma unroll
            for (int q = 0; q < 3; ++q) acc[t][q] = mfma(fa[1][t], fb[1][q], acc[t][q]);
        SBAR();
        LGKM0_BARRIER();
    }
    STAMP(3);
    {
        f32x4 sp[3][3];
#pragma unroll
        for (int kt = 0; kt < 3; ++kt)
#pragma unroll
            for (int q = 0; q < 3; ++q) sp[kt][q] = f32x4{0.f, 0.f, 0.f, 0.f};
#pragma unroll
        for (int u = 0; u < 3; ++u)
#pragma unroll
            for (int q = 0; q < 3; ++q) {
                const bf16x8 qf = cat(pk4(acc[2 * u][q]), pk4(acc[2 * u + 1][q]));
#pragma unroll
                for (int kt = 0; kt < 3; ++kt) sp[kt][q] = mfma(cat(kf[kt][u][0], kf[kt][u][1]), qf, sp[kt][q]);
            }
        LAS char* X = (LAS char*)smem + 2 * SLOT;
#pragma unroll
        for (int kt = 0; kt < 3; ++kt)
#pragma unroll
            for (int q = 0; q < 3; ++q) *(LAS f32x4*)(X + w * 9216 + (kt * 3 + q) * 1024 + lane * 16) = sp[kt][q];
        LGKM0_BARRIER();
#pragma unroll
        for (int kt = 0; kt < 3; ++kt)
#pragma unroll
            for (int q = 0; q < 3; ++q) sp[kt][q] += *(LAS const f32x4*)(X + (w ^ 1) * 9216 + (kt * 3 + q) * 1024 + lane * 16);
        constexpr float C2 = 0.07216878364870322f * 1.4426950408889634f;
        const int pbase = (bid * 2 + bi) * 3;
#pragma unroll
        for (int q = 0; q < 3; ++q) {
            float m = __builtin_fmaxf(__builtin_fmaxf(sp[0][q][0], sp[0][q][1]), __builtin_fmaxf(sp[0][q][2], sp[0][q][3]));
#pragma unroll
            for (int kt = 1; kt < 3; ++kt)
                m = __builtin_fmaxf(m, __builtin_fmaxf(__builtin_fmaxf(sp[kt][q][0], sp[kt][q][1]), __builtin_fmaxf(sp[kt][q][2], sp[kt][q][3])));
            m = xrow_max(m);
            const float nm = -m * C2;
            float sum = 0.f;
#pragma unroll
            for (int kt = 0; kt < 3; ++kt)
#pragma unroll
                for (int r = 0; r < 4; ++r) { const float e = __builtin_amdgcn_exp2f(__builtin_fmaf(sp[kt][q][r], C2, nm)); sp[kt][q][r] = e; sum += e; }
            sum = xrow_sum(sum);
            const float inv = 1.0f / sum;
#pragma unroll
            for (int kt = 0; kt < 3; ++kt) sp[kt][q] *= inv;
            if (hf == 0) {
                const v2u a = pk4(sp[0][q]), b = pk4(sp[1][q]);
                __builtin_amdgcn_raw_buffer_store_b128(v4u{a.x, a.y, b.x, b.y}, wsr, WS_P32 + (pbase + q) * 1024 + lane * 16, 0, 16);
            } else {
                __builtin_amdgcn_raw_buffer_store_b64(pk4(sp[2][q]), wsr, WS_P16 + (pbase + q) * 512 + lane * 8, 0, 16);
            }
        }
    }
    int woff[5];
#pragma unroll
    for (int nt = 0; nt < 5; ++nt) {
        const int NT = 5 * hf + nt;
        woff[nt] = (g >> 1) * 5120 + (NT >> 1) * 1024 + ((g & 1) * 8 + q4) * 64 + (((2 * (NT & 1) + (p4 >> 1)) ^ ((g & 1) << 1)) << 4) + (p4 & 1) * 8;
    }
    bf16x8 va[2][3], wb[2][5];
    auto read2 = [&](int slot, int ss, bf16x8* A, bf16x8* B) {
#pragma unroll
        for (int kt = 0; kt < 3; ++kt) A[kt] = ld128(L + slot + V_OFF + boff[ss] + kt * 2048);
#pragma unroll
        for (int nt = 0; nt < 5; ++nt) B[nt] = cat(tr_read(L + slot + woff[nt] + ss * 10240), tr_read(L + slot + woff[nt] + ss * 10240 + 256));
    };
    read2((11 & 3) * SLOT, 0, va[0], wb[0]);
    SBAR();
    LGKM0_BARRIER();
    STAMP(4);
    v2u vw[4][3][5];
    v4u p32[4][3]; v2u p16[4][3];
#pragma unroll
    for (int hh = 0; hh < 4; ++hh) {
        f32x4 a2[3][5];
#pragma unroll
        for (int kt = 0; kt < 3; ++kt)
#pragma unroll
            for (int nt = 0; nt < 5; ++nt) a2[kt][nt] = f32x4{0.f, 0.f, 0.f, 0.f};
#pragma unroll
        for (int sub = 0; sub < 3; ++sub) {
            const int k = 11 + 3 * hh + sub;
            const int slot = (k & 3) * SLOT, nslot = ((k + 1) & 3) * SLOT;
            if (k == 14 && tid == 0) __hip_atomic_store((gu64*)(ws + WS_FLAGP) + (size_t)(p * 32 + h) * 16, tag, RLX_AGENT);
            read2(slot, 1, va[1], wb[1]);
#pragma unroll
            for (int kt = 0; kt < 3; ++kt)
#pragma unroll
                for (int nt = 0; nt < 5; ++nt) a2[kt][nt] = mfma(va[0][kt], wb[0][nt], a2[kt][nt]);
            if (k < 22) read2(nslot, 0, va[0], wb[0]);
#pragma unroll
            for (int kt = 0; kt < 3; ++kt)
#pragma unroll
                for (int nt = 0; nt < 5; ++nt) a2[kt][nt] = mfma(va[1][kt], wb[1][nt], a2[kt][nt]);
            if (k == 13) asm volatile("s_waitcnt vmcnt(0)" ::: "memory");
            if (k < 22) { SBAR(); LGKM0_BARRIER(); }
        }
#pragma unroll
        for (int kt = 0; kt < 3; ++kt)
#pragma unroll
            for (int nt = 0; nt < 5; ++nt) vw[hh][kt][nt] = pk4(a2[kt][nt]);
    }
    STAMP(5);
    {
        v2u hold = {0u, 0u};
        const int obase = WS_PART + (bid * 4 + w) * 8192 + lane * 16;
#pragma unroll
        for (int q = 0; q < 3; ++q) {
#pragma unroll
            for (int hh = 0; hh < 4; ++hh) {
                const int src = (p * 32 + 4 * s + ((hh + hrot) & 3));
                const int sb = (((src >> 5) * 32 + ((src & 3) * 8) + ((src >> 2) & 7)) * 2 + bi) * 3 + q;
                p32[hh][q] = __builtin_amdgcn_raw_buffer_load_b128(wsr, WS_P32 + sb * 1024 + lane * 16, 0, 16);
                p16[hh][q] = __builtin_amdgcn_raw_buffer_load_b64(wsr, WS_P16 + sb * 512 + lane * 8, 0, 16);
            }
        }
#pragma unroll
        for (int q = 0; q < 3; ++q) {
            f32x4 o[5];
#pragma unroll
            for (int nt = 0; nt < 5; ++nt) o[nt] = f32x4{0.f, 0.f, 0.f, 0.f};
#pragma unroll
            for (int hh = 0; hh < 4; ++hh)
#pragma unroll
                for (int nt = 0; nt < 5; ++nt) o[nt] = mfma(cat(vw[hh][0][nt], vw[hh][1][nt]), __builtin_bit_cast(bf16x8, p32[hh][q]), o[nt]);
#pragma unroll
            for (int hp = 0; hp < 2; ++hp)
#pragma unroll
                for (int nt = 0; nt < 5; ++nt) o[nt] = mfma(cat(vw[2 * hp][2][nt], vw[2 * hp + 1][2][nt]), cat(p16[2 * hp][q], p16[2 * hp + 1][q]), o[nt]);
#pragma unroll
            for (int nt = 0; nt < 5; ++nt) {
                const int t = 5 * q + nt;
                const v2u v = pk4(o[nt]);
                if (t == 14) __builtin_amdgcn_raw_buffer_store_b128(v4u{v.x, v.y, 0u, 0u}, wsr, obase + 7 * 1024, 0, 16);
                else if (t & 1) __builtin_amdgcn_raw_buffer_store_b128(v4u{hold.x, hold.y, v.x, v.y}, wsr, obase + (t >> 1) * 1024, 0, 16);
                else hold = v;
            }
        }
    }
    STAMP(6);
    asm volatile("s_waitcnt vmcnt(0)" ::: "memory");
    STAMP(7);
    BARRIER();
    BARRIER();
    STAMP(8);
    {
        v4u a[8];
#pragma unroll
        for (int m = 0; m < 8; ++m) {
            const int sb = p * 32 + j * 8 + m;
            a[m] = __builtin_amdgcn_raw_buffer_load_b128(wsr, WS_PART + (sb * 4 + w) * 8192 + s * 1024 + lane * 16, 0, 16);
        }
        float r[8];
#pragma unroll
        for (int i = 0; i < 8; ++i) r[i] = 0.f;
#pragma unroll
        for (int m = 0; m < 8; ++m)
#pragma unroll
            for (int i = 0; i < 4; ++i) { r[2 * i] += __uint_as_float(a[m][i] << 16); r[2 * i + 1] += __uint_as_float(a[m][i] & 0xffff0000u); }
        const int t0 = 2 * s, t1 = 2 * s + 1;
        {
            const int q = t0 / 5, nt = t0 - 5 * q;
            u16* o = out + (size_t)((2 * p + bi) * 48 + 16 * q + l15) * DM + 160 * j + 80 * hf + 16 * nt + 4 * g;
            *(GAS v2u*)o = v2u{pk2(r[0], r[1]), pk2(r[2], r[3])};
        }
        if (t1 < 15) {
            const int q = t1 / 5, nt = t1 - 5 * q;
            u16* o = out + (size_t)((2 * p + bi) * 48 + 16 * q + l15) * DM + 160 * j + 80 * hf + 16 * nt + 4 * g;
            *(GAS v2u*)o = v2u{pk2(r[4], r[5]), pk2(r[6], r[7])};
        }
    }
    STAMP(9);
    asm volatile("s_waitcnt vmcnt(0)" ::: "memory");
    STAMP(10);
    STAMP_OUT(0);
}

__global__ void k_amp(char* ws,int wv,int idx,int mult){
  const u64* st=(const u64*)(ws+WS_END);
  const int b=threadIdx.x;
  u64 t0=~0ull; for(int w=0;w<8;++w){ u64 v=st[(size_t)(b*8+w)*NST]; t0=v<t0?v:t0; }
  u64 t1=st[(size_t)(b*8+wv)*NST+idx];
  __shared__ u64 s0[256],s1[256];
  s0[b]=t0; s1[b]=t1; __syncthreads();
  if(b==0){
    u64 mn=~0ull,mx=0; double sum=0; for(int i=0;i<256;++i){ mn=s0[i]<mn?s0[i]:mn; }
    for(int i=0;i<256;++i){ mx=s1[i]>mx?s1[i]:mx; sum+=(double)(s1[i]-mn); }
    u64 d = AMP_AVG ? (u64)(sum/256) : (mx-mn);
    if(d>20000) d=20000;
    u64 a=__builtin_amdgcn_s_memrealtime();
    while(__builtin_amdgcn_s_memrealtime()-a < d*mult) __builtin_amdgcn_s_sleep(4);
  }
}

extern "C" void kernel_launch(void* const* d_in, const int* in_sizes, int n_in, void* d_out, int out_size, void* d_ws, size_t ws_size,
                              hipStream_t stream) {
    (void)in_sizes; (void)n_in; (void)out_size; (void)ws_size;
    hipFuncSetAttribute((const void*)mha_fused, hipFuncAttributeMaxDynamicSharedMemorySize, LDS_BYTES);
    mha_fused<<<256, 512, LDS_BYTES, stream>>>((const u16*)d_in[0], (const u16*)d_in[1], (const u16*)d_in[2], (const u16*)d_in[3],
                                               (const u16*)d_in[4], (u16*)d_out, (char*)d_ws);
    k_amp<<<1,256,0,stream>>>((char*)d_ws, AMP_W, AMP_IDX, 50);
}
```

```cpp
#define AMP_W 0
#define AMP_IDX 10
#define AMP_AVG 0
#include <hip/hip_runtime.h>
#include <hip/hip_bf16.h>

typedef unsigned short u16;
typedef unsigned long long u64;
typedef short bf16x8 __attribute__((ext_vector_type(8)));
typedef short s16x4 __attribute__((ext_vector_type(4)));
typedef float f32x4 __attribute__((ext_vector_type(4)));
typedef float f32x2 __attribute__((ext_vector_type(2)));
typedef __bf16 bf16x2_t __attribute__((ext_vector_type(2)));
typedef unsigned v4u __attribute__((ext_vector_type(4)));
typedef unsigned v2u __attribute__((ext_vector_type(2)));
#define GAS __attribute__((address_space(1)))
#define LAS __attribute__((address_space(3)))
#define CAS __attribute__((address_space(4)))
typedef GAS u64 gu64;
#define RLX_AGENT __ATOMIC_RELAXED, __HIP_MEMORY_SCOPE_AGENT

constexpr int NB = 16, SQ = 48, DM = 640, NH = 32, SK = 48, DH = 192, HD = NH * DH;
constexpr int SLOT = 36864;
constexpr int LDS_BYTES = 4 * SLOT;
constexpr int X_OFF = 24576;
constexpr int V_OFF = 20480;
constexpr int WS_EPOCH = 0;
constexpr int WS_FLAGP = 4096;
constexpr int WS_FLAGO = WS_FLAGP + 256 * 128;
constexpr int WS_P32 = 1 << 17;
constexpr int WS_P16 = WS_P32 + 256 * 6144;
constexpr int WS_PART = WS_P16 + 256 * 3072;
constexpr int WS_END = WS_PART + 256 * 32768;
constexpr unsigned SPIN_CAP = 1u << 22;

#define NST 12
#define STAMP(i) do{ st[i]=__builtin_amdgcn_s_memrealtime(); }while(0)
#define STAMP_DECL u64 st[NST]; _Pragma("unroll") for(int i_=0;i_<NST;++i_) st[i_]=0
#define STAMP_OUT(role) do{ if(lane==0){ _Pragma("unroll") for(int i_=0;i_<NST;++i_) ((u64*)(ws+WS_END))[(size_t)(bid*8+w)*NST+i_]=st[i_]; } }while(0)
#define SBAR() __builtin_amdgcn_sched_barrier(0)
#define BARRIER() asm volatile("s_barrier" ::: "memory")
#define LGKM0_BARRIER() asm volatile("s_waitcnt lgkmcnt(0)\n\ts_barrier" ::: "memory")

__device__ __forceinline__ f32x4 mfma(bf16x8 a, bf16x8 b, f32x4 c) { return __builtin_amdgcn_mfma_f32_16x16x32_bf16(a, b, c, 0, 0, 0); }
__device__ __forceinline__ unsigned pk2(float lo, float hi) {
    f32x2 v = {lo, hi};
    return __builtin_bit_cast(unsigned, __builtin_convertvector(v, bf16x2_t));
}
__device__ __forceinline__ v2u pk4(f32x4 v) { return v2u{pk2(v[0], v[1]), pk2(v[2], v[3])}; }
__device__ __forceinline__ bf16x8 cat(v2u lo, v2u hi) { v4u w = {lo.x, lo.y, hi.x, hi.y}; return __builtin_bit_cast(bf16x8, w); }
typedef short v4i16_t __attribute__((ext_vector_type(4)));
__device__ __forceinline__ v2u tr_read(LAS const char* p) {
    return __builtin_bit_cast(v2u, __builtin_amdgcn_ds_read_tr16_b64_v4i16((LAS v4i16_t*)p));
}
__device__ __forceinline__ bf16x8 ld128(LAS const char* p) { return *(LAS const bf16x8*)p; }
__device__ __forceinline__ void dma16(const char* sbase, unsigned voff, unsigned lds) {
    asm volatile("s_mov_b32 m0, %0\n\ts_nop 0\n\tglobal_load_lds_dwordx4 %1, %2" ::"s"(lds), "v"(voff), "s"(sbase) : "memory");
}
__device__ __forceinline__ float xrow_max(float x) {
    auto s = __builtin_amdgcn_permlane16_swap(__float_as_uint(x), __float_as_uint(x), false, false);
    x = __builtin_fmaxf(__uint_as_float(s[0]), __uint_as_float(s[1]));
    auto t = __builtin_amdgcn_permlane32_swap(__float_as_uint(x), __float_as_uint(x), false, false);
    return __builtin_fmaxf(__uint_as_float(t[0]), __uint_as_float(t[1]));
}
__device__ __forceinline__ float xrow_sum(float x) {
    auto s = __builtin_amdgcn_permlane16_swap(__float_as_uint(x), __float_as_uint(x), false, false);
    x = __uint_as_float(s[0]) + __uint_as_float(s[1]);
    auto t = __builtin_amdgcn_permlane32_swap(__float_as_uint(x), __float_as_uint(x), false, false);
    return __uint_as_float(t[0]) + __uint_as_float(t[1]);
}
__device__ __forceinline__ void poll_flags(gu64* base, int stride_words, int n, u64 tag, int lane) {
    for (unsigned spins = 0; spins < SPIN_CAP; ++spins) {
        u64 v = tag;
        if (lane < n) v = __hip_atomic_load(base + lane * stride_words, RLX_AGENT);
        if (__all(v == tag)) break;
        __builtin_amdgcn_s_sleep(1);
    }
    __builtin_amdgcn_fence(__ATOMIC_ACQUIRE, "wavefront");
}

__global__ __launch_bounds__(512, 2) void mha_fused(const u16* __restrict__ x, const u16* __restrict__ kc, const u16* __restrict__ vc,
                                                    const u16* __restrict__ wq, const u16* __restrict__ wo, u16* __restrict__ out, char* __restrict__ ws) {
    extern __shared__ __attribute__((aligned(16))) char smem[];
    const int tid = threadIdx.x, lane = tid & 63;
    const int w = __builtin_amdgcn_readfirstlane(tid >> 6);
    STAMP_DECL; STAMP(0);
    const int bid = blockIdx.x, s = bid & 7, p = bid >> 5, j = (bid >> 3) & 3, h = 4 * s + j;
    const u64 tag = *(CAS const u64*)(ws + WS_EPOCH + p * 128) + 1ull;
    asm volatile("" ::"s"(tag));
    const unsigned lds0 = (unsigned)(uintptr_t)(LAS char*)smem;
    const int rot1 = (p + 3 * j) % 10;
    const int hrot = (p + j) & 3;
    const int drot = (p >> 2);

    if (w >= 4) {
        const int lw = w - 4;
        const int r8 = lane >> 3;
        const unsigned fx = ((lw & 1) << 2) | (r8 >> 1);
        const unsigned voff_wq = r8 * 12288 + (((lane & 7) ^ ((((r8 >> 1) & 1) << 1) | ((lw & 1) << 2))) << 4);
        const unsigned voff_x = r8 * 1280 + (((lane & 7) ^ fx) << 4);
        const unsigned voff_v = r8 * 384 + (((lane & 7) ^ fx) << 4);
        const unsigned voff_wo = (lane >> 2) * 1280 + (((lane & 3) ^ (((lane >> 5) & 1) << 1)) << 4);
        const char* wq_b = (const char*)wq + (size_t)lw * 8 * 12288 + h * 384;
        const char* x_b = (const char*)x + (size_t)(96 * p + 8 * lw) * 1280;
        const char* wo_b = (const char*)wo + (size_t)lw * 16 * 1280 + j * 320;
        auto issue1 = [&](int step) {
            int kk = step + rot1; if (kk >= 10) kk -= 10;
            const unsigned slot = lds0 + (step & 3) * SLOT;
            const char* a = wq_b + (size_t)kk * 64 * 12288;
#pragma unroll
            for (int i = 0; i < 2; ++i)
#pragma unroll
                for (int cb = 0; cb < 3; ++cb) dma16(a + i * 32 * 12288 + cb * 128, voff_wq, slot + ((lw + 4 * i) * 3 + cb) * 1024);
            const char* b = x_b + kk * 128;
#pragma unroll
            for (int i = 0; i < 3; ++i) dma16(b + i * 32 * 1280, voff_x, slot + X_OFF + (lw + 4 * i) * 1024);
        };
        auto issue2 = [&](int step) {
            const int t = step - 11, hh = t / 3;
            int sub = t - 3 * hh + drot; if (sub >= 3) sub -= 3;
            const int hp = 4 * s + ((hh + hrot) & 3);
            const unsigned slot = lds0 + (step & 3) * SLOT;
            const char* a = wo_b + (size_t)(hp * 192 + sub * 64) * 1280;
#pragma unroll
            for (int cb = 0; cb < 5; ++cb) dma16(a + cb * 64, voff_wo, slot + (lw * 5 + cb) * 1024);
#pragma unroll
            for (int i = 0; i < 3; ++i) {
                const int pi = lw + 4 * i, bi = pi >= 6 ? 1 : 0, kr = pi - 6 * bi;
                dma16((const char*)vc + (size_t)(((2 * p + bi) * 32 + hp) * 48 + 8 * kr) * 384 + sub * 128, voff_v, slot + V_OFF + pi * 1024);
            }
        };
        asm volatile("" ::"s"(wq_b), "s"(x_b), "s"(wo_b), "v"(voff_wq));
        STAMP(10);
        issue1(0);
        STAMP(11);
        issue1(1); issue1(2);
        STAMP(1);
        asm volatile("s_waitcnt vmcnt(9)" ::: "memory"); STAMP(2);
        BARRIER();
        for (int k = 0; k <= 21; ++k) {
            const int n = k + 3;
            if (n <= 9) { issue1(n); asm volatile("s_waitcnt vmcnt(9)" ::: "memory"); }
            else if (n >= 11 && n <= 22) { issue2(n); asm volatile("s_waitcnt vmcnt(8)" ::: "memory"); }
            else asm volatile("s_waitcnt vmcnt(0)" ::: "memory");
            if (k == 10) BARRIER();
            if (k == 20 && w == 4) poll_flags((gu64*)(ws + WS_FLAGP) + (size_t)(p * 32 + 4 * s) * 16, 16, 4, tag, lane);
            BARRIER();
            if (k == 9) STAMP(3);
            if (k == 10) STAMP(4);
            if (k == 20) STAMP(5);
        }
        STAMP(6);
        BARRIER();
        STAMP(7);
        if (w == 4) {
            if (lane == 0) __hip_atomic_store((gu64*)(ws + WS_FLAGO) + (size_t)((p * 4 + j) * 8 + s) * 16, tag, RLX_AGENT);
            poll_flags((gu64*)(ws + WS_FLAGO) + (size_t)((p * 4 + j) * 8) * 16, 16, 8, tag, lane);
            if (s == 0 && j == 0 && lane == 0) __hip_atomic_store((gu64*)(ws + WS_EPOCH + p * 128), tag, RLX_AGENT);
        }
        STAMP(8);
        BARRIER();
        STAMP(9);
        STAMP_OUT(1);
        return;
    }

    const int bi = w >> 1, hf = w & 1;
    const int g = lane >> 4, l15 = lane & 15, q4 = (lane >> 2) & 3, p4 = lane & 3;
    const __amdgpu_buffer_rsrc_t wsr = __builtin_amdgcn_make_buffer_rsrc(ws, 0, WS_END, 0x00020000);
    LAS const char* L = (LAS const char*)smem;

    v2u kf[3][3][2];
    {
        const char* kb = (const char*)kc + (size_t)(((2 * p + bi) * 32 + h) * 48 + l15) * 384 + hf * 192 + g * 8;
#pragma unroll
        for (int kt = 0; kt < 3; ++kt)
#pragma unroll
            for (int u = 0; u < 3; ++u) {
                kf[kt][u][0] = *(GAS const v2u*)(kb + kt * 16 * 384 + u * 64);
                kf[kt][u][1] = *(GAS const v2u*)(kb + kt * 16 * 384 + u * 64 + 32);
            }
    }
    f32x4 acc[6][3];
#pragma unroll
    for (int t = 0; t < 6; ++t)
#pragma unroll
        for (int q = 0; q < 3; ++q) acc[t][q] = f32x4{0.f, 0.f, 0.f, 0.f};
    int aoff[6];
    {
        const int f = (((q4 >> 1) & 1) << 1) | ((g & 1) << 2);
#pragma unroll
        for (int t = 0; t < 6; ++t) {
            const int T = 6 * hf + t;
            aoff[t] = g * 3072 + q4 * 128 + (((2 * (T & 3) + (p4 >> 1)) ^ f) << 4) + (p4 & 1) * 8 + (T >> 2) * 1024;
        }
    }
    int boff[2];
#pragma unroll
    for (int ss = 0; ss < 2; ++ss) boff[ss] = (bi * 48 + l15) * 128 + (((4 * ss + g) ^ ((l15 >> 1) & 7)) << 4);

    bf16x8 fa[2][6], fb[2][3];
    auto read1 = [&](int slot, int ss, bf16x8* A, bf16x8* B) {
#pragma unroll
        for (int q = 0; q < 3; ++q) B[q] = ld128(L + slot + X_OFF + boff[ss] + q * 2048);
#pragma unroll
        for (int t = 0; t < 6; ++t) A[t] = cat(tr_read(L + slot + aoff[t] + ss * 12288), tr_read(L + slot + aoff[t] + ss * 12288 + 512));
    };
    STAMP(1);
    BARRIER();
    STAMP(2);
    read1(0, 0, fa[0], fb[0]);
    for (int k = 0; k < 10; ++k) {
        const int slot = (k & 3) * SLOT, nslot = ((k + 1) & 3) * SLOT;
        read1(slot, 1, fa[1], fb[1]);
#pragma unroll
        for (int t = 0; t < 6; ++t)
#pragma unroll
            for (int q = 0; q < 3; ++q) acc[t][q] = mfma(fa[0][t], fb[0][q], acc[t][q]);
        if (k < 9) read1(nslot, 0, fa[0], fb[0]);
#pragma unroll
        for (int t = 0; t < 6; ++t)
#pragma unroll
            for (int q = 0; q < 3; ++q) acc[t][q] = mfma(fa[1][t], fb[1][q], acc[t][q]);
        SBAR();
        LGKM0_BARRIER();
    }
    STAMP(3);
    {
        f32x4 sp[3][3];
#pragma unroll
        for (int kt = 0; kt < 3; ++kt)
#pragma unroll
            for (int q = 0; q < 3; ++q) sp[kt][q] = f32x4{0.f, 0.f, 0.f, 0.f};
#pragma unroll
        for (int u = 0; u < 3; ++u)
#pragma unroll
            for (int q = 0; q < 3; ++q) {
                const bf16x8 qf = cat(pk4(acc[2 * u][q]), pk4(acc[2 * u + 1][q]));
#pragma unroll
                for (int kt = 0; kt < 3; ++kt) sp[kt][q] = mfma(cat(kf[kt][u][0], kf[kt][u][1]), qf, sp[kt][q]);
            }
        LAS char* X = (LAS char*)smem + 2 * SLOT;
#pragma unroll
        for (int kt = 0; kt < 3; ++kt)
#pragma unroll
            for (int q = 0; q < 3; ++q) *(LAS f32x4*)(X + w * 9216 + (kt * 3 + q) * 1024 + lane * 16) = sp[kt][q];
        LGKM0_BARRIER();
#pragma unroll
        for (int kt = 0; kt < 3; ++kt)
#pragma unroll
            for (int q = 0; q < 3; ++q) sp[kt][q] += *(LAS const f32x4*)(X + (w ^ 1) * 9216 + (kt * 3 + q) * 1024 + lane * 16);
        constexpr float C2 = 0.07216878364870322f * 1.4426950408889634f;
        const int pbase = (bid * 2 + bi) * 3;
#pragma unroll
        for (int q = 0; q < 3; ++q) {
            float m = __builtin_fmaxf(__builtin_fmaxf(sp[0][q][0], sp[0][q][1]), __builtin_fmaxf(sp[0][q][2], sp[0][q][3]));
#pragma unroll
            for (int kt = 1; kt < 3; ++kt)
                m = __builtin_fmaxf(m, __builtin_fmaxf(__builtin_fmaxf(sp[kt][q][0], sp[kt][q][1]), __builtin_fmaxf(sp[kt][q][2], sp[kt][q][3])));
            m = xrow_max(m);
            const float nm = -m * C2;
            float sum = 0.f;
#pragma unroll
            for (int kt = 0; kt < 3; ++kt)
#pragma unroll
                for (int r = 0; r < 4; ++r) { const float e = __builtin_amdgcn_exp2f(__builtin_fmaf(sp[kt][q][r], C2, nm)); sp[kt][q][r] = e; sum += e; }
            sum = xrow_sum(sum);
            const float inv = 1.0f / sum;
#pragma unroll
            for (int kt = 0; kt < 3; ++kt) sp[kt][q] *= inv;
            if (hf == 0) {
                const v2u a = pk4(sp[0][q]), b = pk4(sp[1][q]);
                __builtin_amdgcn_raw_buffer_store_b128(v4u{a.x, a.y, b.x, b.y}, wsr, WS_P32 + (pbase + q) * 1024 + lane * 16, 0, 16);
            } else {
                __builtin_amdgcn_raw_buffer_store_b64(pk4(sp[2][q]), wsr, WS_P16 + (pbase + q) * 512 + lane * 8, 0, 16);
            }
        }
    }
    int woff[5];
#pragma unroll
    for (int nt = 0; nt < 5; ++nt) {
        const int NT = 5 * hf + nt;
        woff[nt] = (g >> 1) * 5120 + (NT >> 1) * 1024 + ((g & 1) * 8 + q4) * 64 + (((2 * (NT & 1) + (p4 >> 1)) ^ ((g & 1) << 1)) << 4) + (p4 & 1) * 8;
    }
    bf16x8 va[2][3], wb[2][5];
    auto read2 = [&](int slot, int ss, bf16x8* A, bf16x8* B) {
#pragma unroll
        for (int kt = 0; kt < 3; ++kt) A[kt] = ld128(L + slot + V_OFF + boff[ss] + kt * 2048);
#pragma unroll
        for (int nt = 0; nt < 5; ++nt) B[nt] = cat(tr_read(L + slot + woff[nt] + ss * 10240), tr_read(L + slot + woff[nt] + ss * 10240 + 256));
    };
    read2((11 & 3) * SLOT, 0, va[0], wb[0]);
    SBAR();
    LGKM0_BARRIER();
    STAMP(4);
    v2u vw[4][3][5];
    v4u p32[4][3]; v2u p16[4][3];
#pragma unroll
    for (int hh = 0; hh < 4; ++hh) {
        f32x4 a2[3][5];
#pragma unroll
        for (int kt = 0; kt < 3; ++kt)
#pragma unroll
            for (int nt = 0; nt < 5; ++nt) a2[kt][nt] = f32x4{0.f, 0.f, 0.f, 0.f};
#pragma unroll
        for (int sub = 0; sub < 3; ++sub) {
            const int k = 11 + 3 * hh + sub;
            const int slot = (k & 3) * SLOT, nslot = ((k + 1) & 3) * SLOT;
            if (k == 14 && tid == 0) __hip_atomic_store((gu64*)(ws + WS_FLAGP) + (size_t)(p * 32 + h) * 16, tag, RLX_AGENT);
            read2(slot, 1, va[1], wb[1]);
#pragma unroll
            for (int kt = 0; kt < 3; ++kt)
#pragma unroll
                for (int nt = 0; nt < 5; ++nt) a2[kt][nt] = mfma(va[0][kt], wb[0][nt], a2[kt][nt]);
            if (k < 22) read2(nslot, 0, va[0], wb[0]);
#pragma unroll
            for (int kt = 0; kt < 3; ++kt)
#pragma unroll
                for (int nt = 0; nt < 5; ++nt) a2[kt][nt] = mfma(va[1][kt], wb[1][nt], a2[kt][nt]);
            if (k == 13) asm volatile("s_waitcnt vmcnt(0)" ::: "memory");
            if (k < 22) { SBAR(); LGKM0_BARRIER(); }
        }
#pragma unroll
        for (int kt = 0; kt < 3; ++kt)
#pragma unroll
            for (int nt = 0; nt < 5; ++nt) vw[hh][kt][nt] = pk4(a2[kt][nt]);
    }
    STAMP(5);
    {
        v2u hold = {0u, 0u};
        const int obase = WS_PART + (bid * 4 + w) * 8192 + lane * 16;
#pragma unroll
        for (int q = 0; q < 3; ++q) {
#pragma unroll
            for (int hh = 0; hh < 4; ++hh) {
                const int src = (p * 32 + 4 * s + ((hh + hrot) & 3));
                const int sb = (((src >> 5) * 32 + ((src & 3) * 8) + ((src >> 2) & 7)) * 2 + bi) * 3 + q;
                p32[hh][q] = __builtin_amdgcn_raw_buffer_load_b128(wsr, WS_P32 + sb * 1024 + lane * 16, 0, 16);
                p16[hh][q] = __builtin_amdgcn_raw_buffer_load_b64(wsr, WS_P16 + sb * 512 + lane * 8, 0, 16);
            }
        }
#pragma unroll
        for (int q = 0; q < 3; ++q) {
            f32x4 o[5];
#pragma unroll
            for (int nt = 0; nt < 5; ++nt) o[nt] = f32x4{0.f, 0.f, 0.f, 0.f};
#pragma unroll
            for (int hh = 0; hh < 4; ++hh)
#pragma unroll
                for (int nt = 0; nt < 5; ++nt) o[nt] = mfma(cat(vw[hh][0][nt], vw[hh][1][nt]), __builtin_bit_cast(bf16x8, p32[hh][q]), o[nt]);
#pragma unroll
            for (int hp = 0; hp < 2; ++hp)
#pragma unroll
                for (int nt = 0; nt < 5; ++nt) o[nt] = mfma(cat(vw[2 * hp][2][nt], vw[2 * hp + 1][2][nt]), cat(p16[2 * hp][q], p16[2 * hp + 1][q]), o[nt]);
#pragma unroll
            for (int nt = 0; nt < 5; ++nt) {
                const int t = 5 * q + nt;
                const v2u v = pk4(o[nt]);
                if (t == 14) __builtin_amdgcn_raw_buffer_store_b128(v4u{v.x, v.y, 0u, 0u}, wsr, obase + 7 * 1024, 0, 16);
                else if (t & 1) __builtin_amdgcn_raw_buffer_store_b128(v4u{hold.x, hold.y, v.x, v.y}, wsr, obase + (t >> 1) * 1024, 0, 16);
                else hold = v;
            }
        }
    }
    STAMP(6);
    asm volatile("s_waitcnt vmcnt(0)" ::: "memory");
    STAMP(7);
    BARRIER();
    BARRIER();
    STAMP(8);
    {
        v4u a[8];
#pragma unroll
        for (int m = 0; m < 8; ++m) {
            const int sb = p * 32 + j * 8 + m;
            a[m] = __builtin_amdgcn_raw_buffer_load_b128(wsr, WS_PART + (sb * 4 + w) * 8192 + s * 1024 + lane * 16, 0, 16);
        }
        float r[8];
#pragma unroll
        for (int i = 0; i < 8; ++i) r[i] = 0.f;
#pragma unroll
        for (int m = 0; m < 8; ++m)
#pragma unroll
            for (int i = 0; i < 4; ++i) { r[2 * i] += __uint_as_float(a[m][i] << 16); r[2 * i + 1] += __uint_as_float(a[m][i] & 0xffff0000u); }
        const int t0 = 2 * s, t1 = 2 * s + 1;
        {
            const int q = t0 / 5, nt = t0 - 5 * q;
            u16* o = out + (size_t)((2 * p + bi) * 48 + 16 * q + l15) * DM + 160 * j + 80 * hf + 16 * nt + 4 * g;
            *(GAS v2u*)o = v2u{pk2(r[0], r[1]), pk2(r[2], r[3])};
        }
        if (t1 < 15) {
            const int q = t1 / 5, nt = t1 - 5 * q;
            u16* o = out + (size_t)((2 * p + bi) * 48 + 16 * q + l15) * DM + 160 * j + 80 * hf + 16 * nt + 4 * g;
            *(GAS v2u*)o = v2u{pk2(r[4], r[5]), pk2(r[6], r[7])};
        }
    }
    STAMP(9);
    asm volatile("s_waitcnt vmcnt(0)" ::: "memory");
    STAMP(10);
    STAMP_OUT(0);
}

__global__ void k_amp(char* ws,int wv,int idx,int mult){
  u64 a0=__builtin_amdgcn_s_memrealtime();
  const u64* st=(const u64*)(ws+WS_END);
  const int b=threadIdx.x;
  u64 t1=0; for(int w=0;w<8;++w) for(int i=0;i<NST;++i){ u64 v=st[(size_t)(b*8+w)*NST+i]; t1=v>t1?v:t1; }
  __shared__ u64 s1[256];
  s1[b]=t1; __syncthreads();
  if(b==0){
    u64 mx=0; for(int i=0;i<256;++i){ mx=s1[i]>mx?s1[i]:mx; }
    u64 d = a0-mx; if(d>20000) d=20000;
    u64 a=__builtin_amdgcn_s_memrealtime();
    while(__builtin_amdgcn_s_memrealtime()-a < d*mult) __builtin_amdgcn_s_sleep(4);
  }
}

extern "C" void kernel_launch(void* const* d_in, const int* in_sizes, int n_in, void* d_out, int out_size, void* d_ws, size_t ws_size,
                              hipStream_t stream) {
    (void)in_sizes; (void)n_in; (void)out_size; (void)ws_size;
    hipFuncSetAttribute((const void*)mha_fused, hipFuncAttributeMaxDynamicSharedMemorySize, LDS_BYTES);
    mha_fused<<<256, 512, LDS_BYTES, stream>>>((const u16*)d_in[0], (const u16*)d_in[1], (const u16*)d_in[2], (const u16*)d_in[3],
                                               (const u16*)d_in[4], (u16*)d_out, (char*)d_ws);
    k_amp<<<1,256,0,stream>>>((char*)d_ws, AMP_W, AMP_IDX, 0);
}
```

```cpp
#define AMP_W 0
#define AMP_IDX 10
#define AMP_AVG 0
#include <hip/hip_runtime.h>
#include <hip/hip_bf16.h>

typedef unsigned short u16;
typedef unsigned long long u64;
typedef short bf16x8 __attribute__((ext_vector_type(8)));
typedef short s16x4 __attribute__((ext_vector_type(4)));
typedef float f32x4 __attribute__((ext_vector_type(4)));
typedef float f32x2 __attribute__((ext_vector_type(2)));
typedef __bf16 bf16x2_t __attribute__((ext_vector_type(2)));
typedef unsigned v4u __attribute__((ext_vector_type(4)));
typedef unsigned v2u __attribute__((ext_vector_type(2)));
#define GAS __attribute__((address_space(1)))
#define LAS __attribute__((address_space(3)))
#define CAS __attribute__((address_space(4)))
typedef GAS u64 gu64;
#define RLX_AGENT __ATOMIC_RELAXED, __HIP_MEMORY_SCOPE_AGENT

constexpr int NB = 16, SQ = 48, DM = 640, NH = 32, SK = 48, DH = 192, HD = NH * DH;
constexpr int SLOT = 36864;
constexpr int LDS_BYTES = 4 * SLOT;
constexpr int X_OFF = 24576;
constexpr int V_OFF = 20480;
constexpr int WS_EPOCH = 0;
constexpr int WS_FLAGP = 4096;
constexpr int WS_FLAGO = WS_FLAGP + 256 * 128;
constexpr int WS_P32 = 1 << 17;
constexpr int WS_P16 = WS_P32 + 256 * 6144;
constexpr int WS_PART = WS_P16 + 256 * 3072;
constexpr int WS_END = WS_PART + 256 * 32768;
constexpr unsigned SPIN_CAP = 1u << 22;

#define NST 12
#define STAMP(i) do{ st[i]=__builtin_amdgcn_s_memrealtime(); }while(0)
#define STAMP_DECL u64 st[NST]; _Pragma("unroll") for(int i_=0;i_<NST;++i_) st[i_]=0
#define STAMP_OUT(role) do{ if(lane==0){ _Pragma("unroll") for(int i_=0;i_<NST;++i_) ((u64*)(ws+WS_END))[(size_t)(bid*8+w)*NST+i_]=st[i_]; } }while(0)
#define SBAR() __builtin_amdgcn_sched_barrier(0)
#define BARRIER() asm volatile("s_barrier" ::: "memory")
#define LGKM0_BARRIER() asm volatile("s_waitcnt lgkmcnt(0)\n\ts_barrier" ::: "memory")

__device__ __forceinline__ f32x4 mfma(bf16x8 a, bf16x8 b, f32x4 c) { return __builtin_amdgcn_mfma_f32_16x16x32_bf16(a, b, c, 0, 0, 0); }
__device__ __forceinline__ unsigned pk2(float lo, float hi) {
    f32x2 v = {lo, hi};
    return __builtin_bit_cast(unsigned, __builtin_convertvector(v, bf16x2_t));
}
__device__ __forceinline__ v2u pk4(f32x4 v) { return v2u{pk2(v[0], v[1]), pk2(v[2], v[3])}; }
__device__ __forceinline__ bf16x8 cat(v2u lo, v2u hi) { v4u w = {lo.x, lo.y, hi.x, hi.y}; return __builtin_bit_cast(bf16x8, w); }
typedef short v4i16_t __attribute__((ext_vector_type(4)));
__device__ __forceinline__ v2u tr_read(LAS const char* p) {
    return __builtin_bit_cast(v2u, __builtin_amdgcn_ds_read_tr16_b64_v4i16((LAS v4i16_t*)p));
}
__device__ __forceinline__ bf16x8 ld128(LAS const char* p) { return *(LAS const bf16x8*)p; }
__device__ __forceinline__ void dma16(const char* sbase, unsigned voff, unsigned lds) {
    asm volatile("s_mov_b32 m0, %0\n\ts_nop 0\n\tglobal_load_lds_dwordx4 %1, %2" ::"s"(lds), "v"(voff), "s"(sbase) : "memory");
}
__device__ __forceinline__ float xrow_max(float x) {
    auto s = __builtin_amdgcn_permlane16_swap(__float_as_uint(x), __float_as_uint(x), false, false);
    x = __builtin_fmaxf(__uint_as_float(s[0]), __uint_as_float(s[1]));
    auto t = __builtin_amdgcn_permlane32_swap(__float_as_uint(x), __float_as_uint(x), false, false);
    return __builtin_fmaxf(__uint_as_float(t[0]), __uint_as_float(t[1]));
}
__device__ __forceinline__ float xrow_sum(float x) {
    auto s = __builtin_amdgcn_permlane16_swap(__float_as_uint(x), __float_as_uint(x), false, false);
    x = __uint_as_float(s[0]) + __uint_as_float(s[1]);
    auto t = __builtin_amdgcn_permlane32_swap(__float_as_uint(x), __float_as_uint(x), false, false);
    return __uint_as_float(t[0]) + __uint_as_float(t[1]);
}
__device__ __forceinline__ void poll_flags(gu64* base, int stride_words, int n, u64 tag, int lane) {
    for (unsigned spins = 0; spins < SPIN_CAP; ++spins) {
        u64 v = tag;
        if (lane < n) v = __hip_atomic_load(base + lane * stride_words, RLX_AGENT);
        if (__all(v == tag)) break;
        __builtin_amdgcn_s_sleep(1);
    }
    __builtin_amdgcn_fence(__ATOMIC_ACQUIRE, "wavefront");
}

__global__ __launch_bounds__(512, 2) void mha_fused(const u16* __restrict__ x, const u16* __restrict__ kc, const u16* __restrict__ vc,
                                                    const u16* __restrict__ wq, const u16* __restrict__ wo, u16* __restrict__ out, char* __restrict__ ws) {
    extern __shared__ __attribute__((aligned(16))) char smem[];
    const int tid = threadIdx.x, lane = tid & 63;
    const int w = __builtin_amdgcn_readfirstlane(tid >> 6);
    STAMP_DECL; STAMP(0);
    const int bid = blockIdx.x, s = bid & 7, p = bid >> 5, j = (bid >> 3) & 3, h = 4 * s + j;
    const u64 tag = *(CAS const u64*)(ws + WS_EPOCH + p * 128) + 1ull;
    asm volatile("" ::"s"(tag));
    const unsigned lds0 = (unsigned)(uintptr_t)(LAS char*)smem;
    const int rot1 = (p + 3 * j) % 10;
    const int hrot = (p + j) & 3;
    const int drot = (p >> 2);

    if (w >= 4) {
        const int lw = w - 4;
        const int r8 = lane >> 3;
        const unsigned fx = ((lw & 1) << 2) | (r8 >> 1);
        const unsigned voff_wq = r8 * 12288 + (((lane & 7) ^ ((((r8 >> 1) & 1) << 1) | ((lw & 1) << 2))) << 4);
        const unsigned voff_x = r8 * 1280 + (((lane & 7) ^ fx) << 4);
        const unsigned voff_v = r8 * 384 + (((lane & 7) ^ fx) << 4);
        const unsigned voff_wo = (lane >> 2) * 1280 + (((lane & 3) ^ (((lane >> 5) & 1) << 1)) << 4);
        const char* wq_b = (const char*)wq + (size_t)lw * 8 * 12288 + h * 384;
        const char* x_b = (const char*)x + (size_t)(96 * p + 8 * lw) * 1280;
        const char* wo_b = (const char*)wo + (size_t)lw * 16 * 1280 + j * 320;
        auto issue1 = [&](int step) {
            int kk = step + rot1; if (kk >= 10) kk -= 10;
            const unsigned slot = lds0 + (step & 3) * SLOT;
            const char* a = wq_b + (size_t)kk * 64 * 12288;
#pragma unroll
            for (int i = 0; i < 2; ++i)
#pragma unroll
                for (int cb = 0; cb < 3; ++cb) dma16(a + i * 32 * 12288 + cb * 128, voff_wq, slot + ((lw + 4 * i) * 3 + cb) * 1024);
            const char* b = x_b + kk * 128;
#pragma unroll
            for (int i = 0; i < 3; ++i) dma16(b + i * 32 * 1280, voff_x, slot + X_OFF + (lw + 4 * i) * 1024);
        };
        auto issue2 = [&](int step) {
            const int t = step - 11, hh = t / 3;
            int sub = t - 3 * hh + drot; if (sub >= 3) sub -= 3;
            const int hp = 4 * s + ((hh + hrot) & 3);
            const unsigned slot = lds0 + (step & 3) * SLOT;
            const char* a = wo_b + (size_t)(hp * 192 + sub * 64) * 1280;
#pragma unroll
            for (int cb = 0; cb < 5; ++cb) dma16(a + cb * 64, voff_wo, slot + (lw * 5 + cb) * 1024);
#pragma unroll
            for (int i = 0; i < 3; ++i) {
                const int pi = lw + 4 * i, bi = pi >= 6 ? 1 : 0, kr = pi - 6 * bi;
                dma16((const char*)vc + (size_t)(((2 * p + bi) * 32 + hp) * 48 + 8 * kr) * 384 + sub * 128, voff_v, slot + V_OFF + pi * 1024);
            }
        };
        asm volatile("" ::"s"(wq_b), "s"(x_b), "s"(wo_b), "v"(voff_wq));
        STAMP(10);
        issue1(0);
        STAMP(11);
        issue1(1); issue1(2);
        STAMP(1);
        asm volatile("s_waitcnt vmcnt(9)" ::: "memory"); STAMP(2);
        BARRIER();
        for (int k = 0; k <= 21; ++k) {
            const int n = k + 3;
            if (n <= 9) { issue1(n); asm volatile("s_waitcnt vmcnt(9)" ::: "memory"); }
            else if (n >= 11 && n <= 22) { issue2(n); asm volatile("s_waitcnt vmcnt(8)" ::: "memory"); }
            else asm volatile("s_waitcnt vmcnt(0)" ::: "memory");
            if (k == 10) BARRIER();
            if (k == 20 && w == 4) poll_flags((gu64*)(ws + WS_FLAGP) + (size_t)(p * 32 + 4 * s) * 16, 16, 4, tag, lane);
            BARRIER();
            if (k == 9) STAMP(3);
            if (k == 10) STAMP(4);
            if (k == 20) STAMP(5);
        }
        STAMP(6);
        BARRIER();
        STAMP(7);
        if (w == 4) {
            if (lane == 0) __hip_atomic_store((gu64*)(ws + WS_FLAGO) + (size_t)((p * 4 + j) * 8 + s) * 16, tag, RLX_AGENT);
            poll_flags((gu64*)(ws + WS_FLAGO) + (size_t)((p * 4 + j) * 8) * 16, 16, 8, tag, lane);
            if (s == 0 && j == 0 && lane == 0) __hip_atomic_store((gu64*)(ws + WS_EPOCH + p * 128), tag, RLX_AGENT);
        }
        STAMP(8);
        BARRIER();
        STAMP(9);
        STAMP_OUT(1);
        return;
    }

    const int bi = w >> 1, hf = w & 1;
    const int g = lane >> 4, l15 = lane & 15, q4 = (lane >> 2) & 3, p4 = lane & 3;
    const __amdgpu_buffer_rsrc_t wsr = __builtin_amdgcn_make_buffer_rsrc(ws, 0, WS_END, 0x00020000);
    LAS const char* L = (LAS const char*)smem;

    v2u kf[3][3][2];
    {
        const char* kb = (const char*)kc + (size_t)(((2 * p + bi) * 32 + h) * 48 + l15) * 384 + hf * 192 + g * 8;
#pragma unroll
        for (int kt = 0; kt < 3; ++kt)
#pragma unroll
            for (int u = 0; u < 3; ++u) {
                kf[kt][u][0] = *(GAS const v2u*)(kb + kt * 16 * 384 + u * 64);
                kf[kt][u][1] = *(GAS const v2u*)(kb + kt * 16 * 384 + u * 64 + 32);
            }
    }
    f32x4 acc[6][3];
#pragma unroll
    for (int t = 0; t < 6; ++t)
#pragma unroll
        for (int q = 0; q < 3; ++q) acc[t][q] = f32x4{0.f, 0.f, 0.f, 0.f};
    int aoff[6];
    {
        const int f = (((q4 >> 1) & 1) << 1) | ((g & 1) << 2);
#pragma unroll
        for (int t = 0; t < 6; ++t) {
            const int T = 6 * hf + t;
            aoff[t] = g * 3072 + q4 * 128 + (((2 * (T & 3) + (p4 >> 1)) ^ f) << 4) + (p4 & 1) * 8 + (T >> 2) * 1024;
        }
    }
    int boff[2];
#pragma unroll
    for (int ss = 0; ss < 2; ++ss) boff[ss] = (bi * 48 + l15) * 128 + (((4 * ss + g) ^ ((l15 >> 1) & 7)) << 4);

    bf16x8 fa[2][6], fb[2][3];
    auto read1 = [&](int slot, int ss, bf16x8* A, bf16x8* B) {
#pragma unroll
        for (int q = 0; q < 3; ++q) B[q] = ld128(L + slot + X_OFF + boff[ss] + q * 2048);
#pragma unroll
        for (int t = 0; t < 6; ++t) A[t] = cat(tr_read(L + slot + aoff[t] + ss * 12288), tr_read(L + slot + aoff[t] + ss * 12288 + 512));
    };
    STAMP(1);
    BARRIER();
    STAMP(2);
    read1(0, 0, fa[0], fb[0]);
    for (int k = 0; k < 10; ++k) {
        const int slot = (k & 3) * SLOT, nslot = ((k + 1) & 3) * SLOT;
        read1(slot, 1, fa[1], fb[1]);
#pragma unroll
        for (int t = 0; t < 6; ++t)
#pragma unroll
            for (int q = 0; q < 3; ++q) acc[t][q] = mfma(fa[0][t], fb[0][q], acc[t][q]);
        if (k < 9) read1(nslot, 0, fa[0], fb[0]);
#pragma unroll
        for (int t = 0; t < 6; ++t)
#pragma unroll
            for (int q = 0; q < 3; ++q) acc[t][q] = mfma(fa[1][t], fb[1][q], acc[t][q]);
        SBAR();
        LGKM0_BARRIER();
    }
    STAMP(3);
    {
        f32x4 sp[3][3];
#pragma unroll
        for (int kt = 0; kt < 3; ++kt)
#pragma unroll
            for (int q = 0; q < 3; ++q) sp[kt][q] = f32x4{0.f, 0.f, 0.f, 0.f};
#pragma unroll
        for (int u = 0; u < 3; ++u)
#pragma unroll
            for (int q = 0; q < 3; ++q) {
                const bf16x8 qf = cat(pk4(acc[2 * u][q]), pk4(acc[2 * u + 1][q]));
#pragma unroll
                for (int kt = 0; kt < 3; ++kt) sp[kt][q] = mfma(cat(kf[kt][u][0], kf[kt][u][1]), qf, sp[kt][q]);
            }
        LAS char* X = (LAS char*)smem + 2 * SLOT;
#pragma unroll
        for (int kt = 0; kt < 3; ++kt)
#pragma unroll
            for (int q = 0; q < 3; ++q) *(LAS f32x4*)(X + w * 9216 + (kt * 3 + q) * 1024 + lane * 16) = sp[kt][q];
        LGKM0_BARRIER();
#pragma unroll
        for (int kt = 0; kt < 3; ++kt)
#pragma unroll
            for (int q = 0; q < 3; ++q) sp[kt][q] += *(LAS const f32x4*)(X + (w ^ 1) * 9216 + (kt * 3 + q) * 1024 + lane * 16);
        constexpr float C2 = 0.07216878364870322f * 1.4426950408889634f;
        const int pbase = (bid * 2 + bi) * 3;
#pragma unroll
        for (int q = 0; q < 3; ++q) {
            float m = __builtin_fmaxf(__builtin_fmaxf(sp[0][q][0], sp[0][q][1]), __builtin_fmaxf(sp[0][q][2], sp[0][q][3]));
#pragma unroll
            for (int kt = 1; kt < 3; ++kt)
                m = __builtin_fmaxf(m, __builtin_fmaxf(__builtin_fmaxf(sp[kt][q][0], sp[kt][q][1]), __builtin_fmaxf(sp[kt][q][2], sp[kt][q][3])));
            m = xrow_max(m);
            const float nm = -m * C2;
            float sum = 0.f;
#pragma unroll
            for (int kt = 0; kt < 3; ++kt)
#pragma unroll
                for (int r = 0; r < 4; ++r) { const float e = __builtin_amdgcn_exp2f(__builtin_fmaf(sp[kt][q][r], C2, nm)); sp[kt][q][r] = e; sum += e; }
            sum = xrow_sum(sum);
            const float inv = 1.0f / sum;
#pragma unroll
            for (int kt = 0; kt < 3; ++kt) sp[kt][q] *= inv;
            if (hf == 0) {
                const v2u a = pk4(sp[0][q]), b = pk4(sp[1][q]);
                __builtin_amdgcn_raw_buffer_store_b128(v4u{a.x, a.y, b.x, b.y}, wsr, WS_P32 + (pbase + q) * 1024 + lane * 16, 0, 16);
            } else {
                __builtin_amdgcn_raw_buffer_store_b64(pk4(sp[2][q]), wsr, WS_P16 + (pbase + q) * 512 + lane * 8, 0, 16);
            }
        }
    }
    int woff[5];
#pragma unroll
    for (int nt = 0; nt < 5; ++nt) {
        const int NT = 5 * hf + nt;
        woff[nt] = (g >> 1) * 5120 + (NT >> 1) * 1024 + ((g & 1) * 8 + q4) * 64 + (((2 * (NT & 1) + (p4 >> 1)) ^ ((g & 1) << 1)) << 4) + (p4 & 1) * 8;
    }
    bf16x8 va[2][3], wb[2][5];
    auto read2 = [&](int slot, int ss, bf16x8* A, bf16x8* B) {
#pragma unroll
        for (int kt = 0; kt < 3; ++kt) A[kt] = ld128(L + slot + V_OFF + boff[ss] + kt * 2048);
#pragma unroll
        for (int nt = 0; nt < 5; ++nt) B[nt] = cat(tr_read(L + slot + woff[nt] + ss * 10240), tr_read(L + slot + woff[nt] + ss * 10240 + 256));
    };
    read2((11 & 3) * SLOT, 0, va[0], wb[0]);
    SBAR();
    LGKM0_BARRIER();
    STAMP(4);
    v2u vw[4][3][5];
    v4u p32[4][3]; v2u p16[4][3];
#pragma unroll
    for (int hh = 0; hh < 4; ++hh) {
        f32x4 a2[3][5];
#pragma unroll
        for (int kt = 0; kt < 3; ++kt)
#pragma unroll
            for (int nt = 0; nt < 5; ++nt) a2[kt][nt] = f32x4{0.f, 0.f, 0.f, 0.f};
#pragma unroll
        for (int sub = 0; sub < 3; ++sub) {
            const int k = 11 + 3 * hh + sub;
            const int slot = (k & 3) * SLOT, nslot = ((k + 1) & 3) * SLOT;
            if (k == 14 && tid == 0) __hip_atomic_store((gu64*)(ws + WS_FLAGP) + (size_t)(p * 32 + h) * 16, tag, RLX_AGENT);
            read2(slot, 1, va[1], wb[1]);
#pragma unroll
            for (int kt = 0; kt < 3; ++kt)
#pragma unroll
                for (int nt = 0; nt < 5; ++nt) a2[kt][nt] = mfma(va[0][kt], wb[0][nt], a2[kt][nt]);
            if (k < 22) read2(nslot, 0, va[0], wb[0]);
#pragma unroll
            for (int kt = 0; kt < 3; ++kt)
#pragma unroll
                for (int nt = 0; nt < 5; ++nt) a2[kt][nt] = mfma(va[1][kt], wb[1][nt], a2[kt][nt]);
            if (k == 13) asm volatile("s_waitcnt vmcnt(0)" ::: "memory");
            if (k < 22) { SBAR(); LGKM0_BARRIER(); }
        }
#pragma unroll
        for (int kt = 0; kt < 3; ++kt)
#pragma unroll
            for (int nt = 0; nt < 5; ++nt) vw[hh][kt][nt] = pk4(a2[kt][nt]);
    }
    STAMP(5);
    {
        v2u hold = {0u, 0u};
        const int obase = WS_PART + (bid * 4 + w) * 8192 + lane * 16;
#pragma unroll
        for (int q = 0; q < 3; ++q) {
#pragma unroll
            for (int hh = 0; hh < 4; ++hh) {
                const int src = (p * 32 + 4 * s + ((hh + hrot) & 3));
                const int sb = (((src >> 5) * 32 + ((src & 3) * 8) + ((src >> 2) & 7)) * 2 + bi) * 3 + q;
                p32[hh][q] = __builtin_amdgcn_raw_buffer_load_b128(wsr, WS_P32 + sb * 1024 + lane * 16, 0, 16);
                p16[hh][q] = __builtin_amdgcn_raw_buffer_load_b64(wsr, WS_P16 + sb * 512 + lane * 8, 0, 16);
            }
        }
#pragma unroll
        for (int q = 0; q < 3; ++q) {
            f32x4 o[5];
#pragma unroll
            for (int nt = 0; nt < 5; ++nt) o[nt] = f32x4{0.f, 0.f, 0.f, 0.f};
#pragma unroll
            for (int hh = 0; hh < 4; ++hh)
#pragma unroll
                for (int nt = 0; nt < 5; ++nt) o[nt] = mfma(cat(vw[hh][0][nt], vw[hh][1][nt]), __builtin_bit_cast(bf16x8, p32[hh][q]), o[nt]);
#pragma unroll
            for (int hp = 0; hp < 2; ++hp)
#pragma unroll
                for (int nt = 0; nt < 5; ++nt) o[nt] = mfma(cat(vw[2 * hp][2][nt], vw[2 * hp + 1][2][nt]), cat(p16[2 * hp][q], p16[2 * hp + 1][q]), o[nt]);
#pragma unroll
            for (int nt = 0; nt < 5; ++nt) {
                const int t = 5 * q + nt;
                const v2u v = pk4(o[nt]);
                if (t == 14) __builtin_amdgcn_raw_buffer_store_b128(v4u{v.x, v.y, 0u, 0u}, wsr, obase + 7 * 1024, 0, 16);
                else if (t & 1) __builtin_amdgcn_raw_buffer_store_b128(v4u{hold.x, hold.y, v.x, v.y}, wsr, obase + (t >> 1) * 1024, 0, 16);
                else hold = v;
            }
        }
    }
    STAMP(6);
    asm volatile("s_waitcnt vmcnt(0)" ::: "memory");
    STAMP(7);
    BARRIER();
    BARRIER();
    STAMP(8);
    {
        v4u a[8];
#pragma unroll
        for (int m = 0; m < 8; ++m) {
            const int sb = p * 32 + j * 8 + m;
            a[m] = __builtin_amdgcn_raw_buffer_load_b128(wsr, WS_PART + (sb * 4 + w) * 8192 + s * 1024 + lane * 16, 0, 16);
        }
        float r[8];
#pragma unroll
        for (int i = 0; i < 8; ++i) r[i] = 0.f;
#pragma unroll
        for (int m = 0; m < 8; ++m)
#pragma unroll
            for (int i = 0; i < 4; ++i) { r[2 * i] += __uint_as_float(a[m][i] << 16); r[2 * i + 1] += __uint_as_float(a[m][i] & 0xffff0000u); }
        const int t0 = 2 * s, t1 = 2 * s + 1;
        {
            const int q = t0 / 5, nt = t0 - 5 * q;
            u16* o = out + (size_t)((2 * p + bi) * 48 + 16 * q + l15) * DM + 160 * j + 80 * hf + 16 * nt + 4 * g;
            *(GAS v2u*)o = v2u{pk2(r[0], r[1]), pk2(r[2], r[3])};
        }
        if (t1 < 15) {
            const int q = t1 / 5, nt = t1 - 5 * q;
            u16* o = out + (size_t)((2 * p + bi) * 48 + 16 * q + l15) * DM + 160 * j + 80 * hf + 16 * nt + 4 * g;
            *(GAS v2u*)o = v2u{pk2(r[4], r[5]), pk2(r[6], r[7])};
        }
    }
    STAMP(9);
    asm volatile("s_waitcnt vmcnt(0)" ::: "memory");
    STAMP(10);
    STAMP_OUT(0);
}

__global__ void k_pre(char* ws){ if(threadIdx.x==0) ((u64*)(ws+WS_END+(1<<20)))[0]=__builtin_amdgcn_s_memrealtime(); }
__global__ void k_amp(char* ws,int wv,int idx,int mult){
  const u64* st=(const u64*)(ws+WS_END);
  const int b=threadIdx.x;
  u64 t0=~0ull; for(int w=0;w<8;++w){ u64 v=st[(size_t)(b*8+w)*NST]; t0=v<t0?v:t0; }
  __shared__ u64 s1[256];
  s1[b]=t0; __syncthreads();
  if(b==0){
    u64 mn=~0ull; for(int i=0;i<256;++i){ mn=s1[i]<mn?s1[i]:mn; }
    u64 d = mn-((u64*)(ws+WS_END+(1<<20)))[0]; if(d>20000) d=20000;
    u64 a=__builtin_amdgcn_s_memrealtime();
    while(__builtin_amdgcn_s_memrealtime()-a < d*mult) __builtin_amdgcn_s_sleep(4);
  }
}

extern "C" void kernel_launch(void* const* d_in, const int* in_sizes, int n_in, void* d_out, int out_size, void* d_ws, size_t ws_size,
                              hipStream_t stream) {
    (void)in_sizes; (void)n_in; (void)out_size; (void)ws_size;
    hipFuncSetAttribute((const void*)mha_fused, hipFuncAttributeMaxDynamicSharedMemorySize, LDS_BYTES);
    k_pre<<<1,64,0,stream>>>((char*)d_ws);
    mha_fused<<<256, 512, LDS_BYTES, stream>>>((const u16*)d_in[0], (const u16*)d_in[1], (const u16*)d_in[2], (const u16*)d_in[3],
                                               (const u16*)d_in[4], (u16*)d_out, (char*)d_ws);
    k_amp<<<1,256,0,stream>>>((char*)d_ws, AMP_W, AMP_IDX, 0);
}
```

```cpp
#include <hip/hip_runtime.h>
#include <hip/hip_bf16.h>

typedef unsigned short u16;
typedef unsigned long long u64;
typedef short bf16x8 __attribute__((ext_vector_type(8)));
typedef float f32x4 __attribute__((ext_vector_type(4)));
typedef float f32x2 __attribute__((ext_vector_type(2)));
typedef __bf16 bf16x2_t __attribute__((ext_vector_type(2)));
typedef unsigned v4u __attribute__((ext_vector_type(4)));
typedef unsigned v2u __attribute__((ext_vector_type(2)));
#define GAS __attribute__((address_space(1)))
#define LAS __attribute__((address_space(3)))
#define CAS __attribute__((address_space(4)))
typedef GAS u64 gu64;
#define RLX_AGENT __ATOMIC_RELAXED, __HIP_MEMORY_SCOPE_AGENT

constexpr int DM = 640;
constexpr int NTHR = 512;
constexpr int SLOT = 36864;
constexpr int L_TAG = 4 * SLOT;
constexpr int L_DUMP = 4 * SLOT + 16;
constexpr int LDS_BYTES = L_DUMP + 256;
constexpr int X_OFF = 24576;
constexpr int V_OFF = 20480;
constexpr int H_OFF = 16384;
constexpr int WS_EPOCH = 0;
constexpr int WS_FLAGP = 4096;
constexpr int WS_FLAGO = WS_FLAGP + 1024 * 128;
constexpr int WS_P32 = 1 << 19;
constexpr int WS_P16 = WS_P32 + 256 * 6144;
constexpr int WS_PART = WS_P16 + 256 * 3072;
constexpr int WS_END = WS_PART + 256 * 32768;
constexpr unsigned CODE_BYTES = 21120;
constexpr unsigned SPIN_CAP = 1u << 17;

#define SBAR() __builtin_amdgcn_sched_barrier(0)
#define BARRIER() asm volatile("s_waitcnt lgkmcnt(0)\n\ts_barrier" ::: "memory")
#define RAW_BARRIER() asm volatile("s_barrier" ::: "memory")
template <int N> __device__ __forceinline__ void wait_vm() { asm volatile("s_waitcnt vmcnt(%0)" ::"n"(N) : "memory"); }

__device__ __forceinline__ f32x4 mfma(bf16x8 a, bf16x8 b, f32x4 c) { return __builtin_amdgcn_mfma_f32_16x16x32_bf16(a, b, c, 0, 0, 0); }
__device__ __forceinline__ unsigned pk2(float lo, float hi) {
    f32x2 v = {lo, hi};
    return __builtin_bit_cast(unsigned, __builtin_convertvector(v, bf16x2_t));
}
__device__ __forceinline__ v2u pk4(f32x4 v) { return v2u{pk2(v[0], v[1]), pk2(v[2], v[3])}; }
__device__ __forceinline__ bf16x8 cat(v2u lo, v2u hi) { v4u w = {lo.x, lo.y, hi.x, hi.y}; return __builtin_bit_cast(bf16x8, w); }
typedef short v4i16_t __attribute__((ext_vector_type(4)));
__device__ __forceinline__ v2u tr_read(LAS const char* p) {
    return __builtin_bit_cast(v2u, __builtin_amdgcn_ds_read_tr16_b64_v4i16((LAS v4i16_t*)p));
}
__device__ __forceinline__ bf16x8 ld128(LAS const char* p) { return *(LAS const bf16x8*)p; }
__device__ __forceinline__ void set_m0(unsigned lds) { asm volatile("s_mov_b32 m0, %0\n\ts_nop 0" ::"s"(lds) : "memory"); }
template <int IMM> __device__ __forceinline__ void dma_imm(const char* sbase, unsigned voff) {
    asm volatile("global_load_lds_dwordx4 %0, %1 offset:%2" ::"v"(voff), "s"(sbase), "n"(IMM) : "memory");
}
__device__ __forceinline__ float xrow_max(float x) {
    auto s = __builtin_amdgcn_permlane16_swap(__float_as_uint(x), __float_as_uint(x), false, false);
    x = __builtin_fmaxf(__uint_as_float(s[0]), __uint_as_float(s[1]));
    auto t = __builtin_amdgcn_permlane32_swap(__float_as_uint(x), __float_as_uint(x), false, false);
    return __builtin_fmaxf(__uint_as_float(t[0]), __uint_as_float(t[1]));
}
__device__ __forceinline__ float xrow_sum(float x) {
    auto s = __builtin_amdgcn_permlane16_swap(__float_as_uint(x), __float_as_uint(x), false, false);
    x = __uint_as_float(s[0]) + __uint_as_float(s[1]);
    auto t = __builtin_amdgcn_permlane32_swap(__float_as_uint(x), __float_as_uint(x), false, false);
    return __uint_as_float(t[0]) + __uint_as_float(t[1]);
}
__device__ __forceinline__ void poll_flags(gu64* base, int stride_words, int n, u64 tag, int lane) {
    for (unsigned spins = 0; spins < SPIN_CAP; ++spins) {
        u64 v = tag;
        if (lane < n) v = __hip_atomic_load(base + lane * stride_words, RLX_AGENT);
        if (__all(v == tag)) break;
        __builtin_amdgcn_s_sleep(1);
    }
    __builtin_amdgcn_fence(__ATOMIC_ACQUIRE, "wavefront");
}

__global__ __launch_bounds__(NTHR, 2) void mha_fused(const u16* __restrict__ x, const u16* __restrict__ kc, const u16* __restrict__ vc,
                                                     const u16* __restrict__ wq, const u16* __restrict__ wo, u16* __restrict__ out, char* __restrict__ ws) {
    extern __shared__ __attribute__((aligned(16))) char smem[];
    const int tid = threadIdx.x, lane = tid & 63;
    const int w = __builtin_amdgcn_readfirstlane(tid >> 6);
    const char* pc0;
    asm volatile("s_getpc_b64 %0" : "=s"(pc0));
    if (w == 0) {
        const unsigned o = lane * 128;
        asm volatile("s_mov_b32 m0, %3\n\ts_nop 0\n\tglobal_load_lds_dword %0, %2\n\tglobal_load_lds_dword %1, %2"
                     ::"v"(o), "v"(o + 8192), "s"(pc0), "s"(L_DUMP) : "memory");
        asm volatile("global_load_lds_dword %0, %1" ::"v"(o + 16384 < CODE_BYTES ? o + 16384 : 0u), "s"(pc0) : "memory");
    }
    const int bid = blockIdx.x, s = bid & 7, p = bid >> 5, j = (bid >> 3) & 3, h = 4 * s + j;
    asm volatile("" ::"s"(x), "s"(kc), "s"(vc), "s"(wq), "s"(wo), "s"(out), "s"(ws));
    const int hrot = (p + j) & 3;
    if (__builtin_expect(w < 4, 0)) {
        if (w == 1) {
            const char* a;
            if (lane < 2) a = (const char*)kc + (size_t)((2 * p + lane) * 32 + h) * 18432;
            else if (lane < 6) a = (const char*)vc + (size_t)((2 * p + ((lane - 2) >> 1)) * 32 + 4 * s) * 18432 + (lane & 1) * 65536;
            else if (lane < 21) a = (const char*)wo + (size_t)(4 * s * 192) * 1280 + (lane - 6) * 65536;
            else if (lane < 23) a = (const char*)out + (size_t)(96 * p) * 1280 + (lane - 21) * 65536;
            else if (lane < 26) a = ws + WS_P32 + p * (32 * 6144) + (lane - 23) * 65536;
            else if (lane < 28) a = ws + WS_P16 + p * (32 * 3072) + (lane - 26) * 65536;
            else if (lane < 44) a = ws + WS_PART + p * (32 * 32768) + (lane - 28) * 65536;
            else a = ws + WS_FLAGP + (lane & 1) * (WS_FLAGO - WS_FLAGP) + p * (32 * 4 * 128);
            asm volatile("s_mov_b32 m0, %1\n\ts_nop 0\n\tglobal_load_lds_dword %0, off sc1" ::"v"(a), "s"(L_DUMP) : "memory");
        }
        if (w == 2) {
            asm volatile("s_mov_b32 m0, %3\n\ts_nop 0\n\tglobal_load_lds_dword %0, %2\n\tglobal_load_lds_dword %1, %2"
                         ::"v"(lane * 65536 + h * 384), "v"((lane < 56 ? lane + 64 : 0) * 65536 + h * 384), "s"(wq), "s"(L_DUMP) : "memory");
        }
        const __amdgpu_buffer_rsrc_t wsr = __builtin_amdgcn_make_buffer_rsrc(ws, 0, WS_END, 0x00020000);
        LAS const char* L = (LAS const char*)smem;
        __builtin_amdgcn_s_setprio(1);
        const int bi = w >> 1, hf = w & 1;
        const int g = lane >> 4, l15 = lane & 15, q4 = (lane >> 2) & 3, p4 = lane & 3;
        const int ftr = (((q4 >> 1) & 1) << 1) | ((g & 1) << 2);

        f32x4 acc[6][3];
#pragma unroll
        for (int t = 0; t < 6; ++t)
#pragma unroll
            for (int q = 0; q < 3; ++q) acc[t][q] = f32x4{0.f, 0.f, 0.f, 0.f};
        LAS const char* pa[4];
#pragma unroll
        for (int t = 0; t < 4; ++t) {
            const int T = 6 * hf + t;
            pa[t] = L + g * 3072 + q4 * 128 + (((2 * (T & 3) + (p4 >> 1)) ^ ftr) << 4) + (p4 & 1) * 8 + (T >> 2) * 1024;
        }
        const int rowoff = (bi * 48 + l15) * 128;
        const int rsw = (l15 >> 1) & 7;
        LAS const char* pb[2];
#pragma unroll
        for (int ss = 0; ss < 2; ++ss) pb[ss] = L + X_OFF + rowoff + (((4 * ss + g) ^ rsw) << 4);
#pragma unroll
        for (int t = 0; t < 4; ++t) asm volatile("" : "+v"(pa[t]));
        asm volatile("" : "+v"(pb[0]), "+v"(pb[1]));

        bf16x8 fa[2][6], fb[2][3];
        const u64 tag = *(CAS const u64*)(ws + WS_EPOCH + p * 128) + 1ull;
        if (w == 0 && lane == 0) *(volatile LAS u64*)(uintptr_t)L_TAG = tag;
        BARRIER();
#pragma unroll
        for (int q = 0; q < 3; ++q) fb[0][q] = ld128(pb[0] + q * 2048);
#pragma unroll
        for (int t = 0; t < 6; ++t) fa[0][t] = cat(tr_read(pa[t & 3] + (t >> 2) * 1024), tr_read(pa[t & 3] + (t >> 2) * 1024 + 512));
        __builtin_amdgcn_s_waitcnt(0xC07F);
        SBAR();
        v2u lo;
#define ADV(ptr) do { ptr += d; asm volatile("" : "+v"(ptr)); } while (0)
#define WAIT_LGKM(n) do { __builtin_amdgcn_s_waitcnt(0xC07F | ((n) << 8)); SBAR(); } while (0)
        constexpr int BAR_AT = 14, BAR_AT2 = 12;
#pragma unroll 1
        for (int k = 0; k < 10; ++k) {
            const int d = (k & 3) == 3 ? -3 * SLOT : SLOT;
#pragma unroll
            for (int i = 0; i < 18; ++i) {
                if (i == 0) WAIT_LGKM(6);
                if (i == 9) WAIT_LGKM(9);
                acc[i / 3][i % 3] = mfma(fa[0][i / 3], fb[0][i % 3], acc[i / 3][i % 3]);
                SBAR();
                if (i < 3) fb[1][i] = ld128(pb[1] + i * 2048);
                else if (i < 15) {
                    const int t = (i - 3) >> 1;
                    LAS const char* q = pa[t & 3] + (t >> 2) * 1024 + 12288;
                    if (((i - 3) & 1) == 0) lo = tr_read(q);
                    else fa[1][t] = cat(lo, tr_read(q + 512));
                }
                if (i == 15) { ADV(pb[0]); ADV(pb[1]); }
                if (i == 16) { ADV(pa[0]); ADV(pa[1]); }
                if (i == 17) { ADV(pa[2]); ADV(pa[3]); }
                SBAR();
            }
            if (k == 0) RAW_BARRIER();
            SBAR();
#pragma unroll
            for (int i = 0; i < 18; ++i) {
                if (i == 0) WAIT_LGKM(6);
                if (i == 9) WAIT_LGKM(9);
                acc[i / 3][i % 3] = mfma(fa[1][i / 3], fb[1][i % 3], acc[i / 3][i % 3]);
                SBAR();
                if (i < 3) fb[0][i] = ld128(pb[0] + i * 2048);
                else if (i < 15) {
                    const int t = (i - 3) >> 1;
                    LAS const char* q = pa[t & 3] + (t >> 2) * 1024;
                    if (((i - 3) & 1) == 0) lo = tr_read(q);
                    else fa[0][t] = cat(lo, tr_read(q + 512));
                }
                if (i == BAR_AT) RAW_BARRIER();
                SBAR();
            }
        }
        LAS const char* pw[5];
        LAS const char* pv[2];
        bf16x8 va[2][3], wb[2][5];
        {
            f32x4 sp[3][3];
            LAS const char* K = L + 2 * SLOT + (bi * 6 + (l15 >> 3)) * 3072 + (l15 & 7) * 128 + (g & 1) * 8;
            bf16x8 kf[3][3];
#pragma unroll
            for (int u = 0; u < 3; ++u) {
                const int c0 = 12 * hf + 4 * u + (g >> 1), c1 = c0 + 2;
                const int a0 = (c0 >> 3) * 1024 + (((c0 & 7) ^ rsw) << 4), a1 = (c1 >> 3) * 1024 + (((c1 & 7) ^ rsw) << 4);
#pragma unroll
                for (int kt = 0; kt < 3; ++kt) kf[u][kt] = cat(*(LAS const v2u*)(K + kt * 6144 + a0), *(LAS const v2u*)(K + kt * 6144 + a1));
            }
#pragma unroll
            for (int kt = 0; kt < 3; ++kt)
#pragma unroll
                for (int q = 0; q < 3; ++q) sp[kt][q] = f32x4{0.f, 0.f, 0.f, 0.f};
#pragma unroll
            for (int u = 0; u < 3; ++u)
#pragma unroll
                for (int q = 0; q < 3; ++q) {
                    const bf16x8 qf = cat(pk4(acc[2 * u][q]), pk4(acc[2 * u + 1][q]));
#pragma unroll
                    for (int kt = 0; kt < 3; ++kt) sp[kt][q] = mfma(kf[u][kt], qf, sp[kt][q]);
                }
            LAS char* X = (LAS char*)smem + 3 * SLOT + lane * 16;
            if (hf == 0) {
#pragma unroll
                for (int kt = 0; kt < 3; ++kt) *(LAS f32x4*)(X + w * 6144 + kt * 1024) = sp[kt][2];
            } else {
#pragma unroll
                for (int kt = 0; kt < 3; ++kt)
#pragma unroll
                    for (int q = 0; q < 2; ++q) *(LAS f32x4*)(X + w * 6144 + (kt * 2 + q) * 1024) = sp[kt][q];
            }
            BARRIER();
#pragma unroll
            for (int nt = 0; nt < 4; ++nt) pw[nt] = L + g * 2048 + hf * 1024 + q4 * 128 + (((2 * nt + (p4 >> 1)) ^ ftr) << 4) + (p4 & 1) * 8;
            pw[4] = L + H_OFF + (g >> 1) * 1024 + ((g & 1) * 8 + q4) * 64 + (((2 * hf + (p4 >> 1)) ^ ((g & 1) << 1)) << 4) + (p4 & 1) * 8;
#pragma unroll
            for (int ss = 0; ss < 2; ++ss) pv[ss] = L + V_OFF + rowoff + (((4 * ss + g) ^ rsw) << 4);
            constexpr float C2 = 0.07216878364870322f * 1.4426950408889634f;
            const int pbase = (bid * 2 + bi) * 3;
            auto softmax = [&](f32x4 s0, f32x4 s1, f32x4 s2, int q) {
                float m = __builtin_fmaxf(__builtin_fmaxf(__builtin_fmaxf(s0[0], s0[1]), __builtin_fmaxf(s0[2], s0[3])),
                                          __builtin_fmaxf(__builtin_fmaxf(s1[0], s1[1]), __builtin_fmaxf(s1[2], s1[3])));
                m = __builtin_fmaxf(m, __builtin_fmaxf(__builtin_fmaxf(s2[0], s2[1]), __builtin_fmaxf(s2[2], s2[3])));
                m = xrow_max(m);
                const float nm = -m * C2;
#pragma unroll
                for (int r = 0; r < 4; ++r) {
                    s0[r] = __builtin_amdgcn_exp2f(__builtin_fmaf(s0[r], C2, nm));
                    s1[r] = __builtin_amdgcn_exp2f(__builtin_fmaf(s1[r], C2, nm));
                    s2[r] = __builtin_amdgcn_exp2f(__builtin_fmaf(s2[r], C2, nm));
                }
                const f32x4 t = s0 + s1 + s2;
                const float inv = __builtin_amdgcn_rcpf(xrow_sum((t[0] + t[1]) + (t[2] + t[3])));
                const v2u a = pk4(s0 * inv), b = pk4(s1 * inv);
                __builtin_amdgcn_raw_buffer_store_b128(v4u{a.x, a.y, b.x, b.y}, wsr, WS_P32 + (pbase + q) * 1024 + lane * 16, 0, 16);
                __builtin_amdgcn_raw_buffer_store_b64(pk4(s2 * inv), wsr, WS_P16 + (pbase + q) * 512 + lane * 8, 0, 16);
            };
            LAS const char* Y = L + 3 * SLOT + (w ^ 1) * 6144 + lane * 16;
            f32x4 y[6];
            if (hf == 0) {
#pragma unroll
                for (int i = 0; i < 6; ++i) y[i] = *(LAS const f32x4*)(Y + i * 1024);
            } else {
#pragma unroll
                for (int i = 0; i < 3; ++i) y[i] = *(LAS const f32x4*)(Y + i * 1024);
            }
            if (hf == 0) {
                softmax(sp[0][0] + y[0], sp[1][0] + y[2], sp[2][0] + y[4], 0);
                softmax(sp[0][1] + y[1], sp[1][1] + y[3], sp[2][1] + y[5], 1);
            } else {
                softmax(sp[0][2] + y[0], sp[1][2] + y[1], sp[2][2] + y[2], 2);
            }
            BARRIER();
#pragma unroll
            for (int kt = 0; kt < 3; ++kt) va[0][kt] = ld128(pv[0] + kt * 2048);
#pragma unroll
            for (int nt = 0; nt < 4; ++nt) wb[0][nt] = cat(tr_read(pw[nt]), tr_read(pw[nt] + 512));
            wb[0][4] = cat(tr_read(pw[4]), tr_read(pw[4] + 256));
            RAW_BARRIER();
            __builtin_amdgcn_s_waitcnt(0xC07F);
            SBAR();
        }
        v2u vw[4][3][5];
        f32x4 a2[3][5];
        int kstep = 12;
#define T2_A(FIRST, PREV)                                                                                         \
            {                                                                                                         \
                const int d = (kstep & 3) == 3 ? -3 * SLOT : SLOT;                                                    \
                _Pragma("unroll") for (int i = 0; i < 15; ++i) {                                                      \
                    if (i == 0) WAIT_LGKM(6);                                                                         \
                    if (i == 6) WAIT_LGKM(6);                                                                         \
                    if (FIRST) {                                                                                      \
                        if ((PREV) >= 0) { vw[(PREV) < 0 ? 0 : (PREV)][i % 3][i / 3] = pk4(a2[i % 3][i / 3]); asm volatile("" : "+v"(vw[(PREV) < 0 ? 0 : (PREV)][i % 3][i / 3])); } \
                        a2[i % 3][i / 3] = mfma(va[0][i % 3], wb[0][i / 3], f32x4{0.f, 0.f, 0.f, 0.f});               \
                    } else a2[i % 3][i / 3] = mfma(va[0][i % 3], wb[0][i / 3], a2[i % 3][i / 3]);                     \
                    SBAR();                                                                                           \
                    if (i < 3) va[1][i] = ld128(pv[1] + i * 2048);                                                    \
                    else if (i < 13) {                                                                                \
                        const int nt = (i - 3) >> 1;                                                                  \
                        if (((i - 3) & 1) == 0) lo = tr_read(pw[nt] + (nt < 4 ? 8192 : 2048));                        \
                        else wb[1][nt] = cat(lo, tr_read(pw[nt] + (nt < 4 ? 8192 + 512 : 2048 + 256)));               \
                    }                                                                                                 \
                    if (i == 1) ADV(pv[0]);                                                                           \
                    if (i == 4) ADV(pv[1]);                                                                           \
                    if (i == 7) ADV(pw[0]);                                                                           \
                    if (i == 13) { ADV(pw[1]); ADV(pw[2]); }                                                          \
                    if (i == 14) { ADV(pw[3]); ADV(pw[4]); }                                                          \
                    SBAR();                                                                                           \
                }                                                                                                     \
            }
#define T2_B(READ)                                                                                                \
            _Pragma("unroll") for (int i = 0; i < 15; ++i) {                                                          \
                if (i == 0) WAIT_LGKM(6);                                                                             \
                if (i == 6) { if (READ) WAIT_LGKM(6); else WAIT_LGKM(0); }                                            \
                a2[i % 3][i / 3] = mfma(va[1][i % 3], wb[1][i / 3], a2[i % 3][i / 3]);                                \
                SBAR();                                                                                               \
                if (!(READ)) {}                                                                                       \
                else if (i < 3) va[0][i] = ld128(pv[0] + i * 2048);                                                   \
                else if (i < 13) {                                                                                    \
                    const int nt = (i - 3) >> 1;                                                                      \
                    if (((i - 3) & 1) == 0) lo = tr_read(pw[nt]);                                                     \
                    else wb[0][nt] = cat(lo, tr_read(pw[nt] + (nt < 4 ? 512 : 256)));                                 \
                }                                                                                                     \
                if ((READ) && i == BAR_AT2) RAW_BARRIER();                                            \
                SBAR();                                                                                               \
            }
#pragma unroll
        for (int hh = 0; hh < 4; ++hh) {
            T2_A(true, hh - 1)
#pragma unroll 1
            for (int sub = 0; sub < 2; ++sub) {
                T2_B(true)
                ++kstep;
                T2_A(false, -1)
            }
            T2_B(hh < 3)
            ++kstep;
            if (hh == 0) {
                wait_vm<0>();
                if (lane == 0) __hip_atomic_store((gu64*)(ws + WS_FLAGP) + (size_t)(bid * 4 + w) * 16, tag, RLX_AGENT);
            }
        }
#pragma unroll
        for (int kt = 0; kt < 3; ++kt)
#pragma unroll
            for (int nt = 0; nt < 5; ++nt) vw[3][kt][nt] = pk4(a2[kt][nt]);
        RAW_BARRIER();
        {
            LAS const char* P = L + bi * 18432;
            v2u hold = {0u, 0u};
            const int obase = WS_PART + (bid * 4 + w) * 8192 + lane * 16;
#pragma unroll
            for (int q = 0; q < 3; ++q) {
                bf16x8 p32[4]; v2u p16[4];
#pragma unroll
                for (int hh = 0; hh < 4; ++hh) {
                    p32[hh] = ld128(P + (hh * 3 + q) * 1024 + lane * 16);
                    p16[hh] = *(LAS const v2u*)(P + 12288 + (hh * 3 + q) * 512 + lane * 8);
                }
                f32x4 o[5];
#pragma unroll
                for (int nt = 0; nt < 5; ++nt) o[nt] = f32x4{0.f, 0.f, 0.f, 0.f};
#pragma unroll
                for (int hh = 0; hh < 4; ++hh)
#pragma unroll
                    for (int nt = 0; nt < 5; ++nt) o[nt] = mfma(cat(vw[hh][0][nt], vw[hh][1][nt]), p32[hh], o[nt]);
#pragma unroll
                for (int hp = 0; hp < 2; ++hp)
#pragma unroll
                    for (int nt = 0; nt < 5; ++nt) o[nt] = mfma(cat(vw[2 * hp][2][nt], vw[2 * hp + 1][2][nt]), cat(p16[2 * hp], p16[2 * hp + 1]), o[nt]);
#pragma unroll
                for (int nt = 0; nt < 5; ++nt) {
                    const int t = 5 * q + nt;
                    const v2u v = pk4(o[nt]);
                    if (t == 14) __builtin_amdgcn_raw_buffer_store_b128(v4u{v.x, v.y, 0u, 0u}, wsr, obase + 7 * 1024, 0, 16);
                    else if (t & 1) __builtin_amdgcn_raw_buffer_store_b128(v4u{hold.x, hold.y, v.x, v.y}, wsr, obase + (t >> 1) * 1024, 0, 16);
                    else hold = v;
                }
            }
        }
        wait_vm<0>();
        if (lane == 0) __hip_atomic_store((gu64*)(ws + WS_FLAGO) + (size_t)(bid * 4 + w) * 16, tag, RLX_AGENT);
        poll_flags((gu64*)(ws + WS_FLAGO) + (size_t)((bid - s) * 4 + w) * 16, 4 * 16, 8, tag, lane);
        {
            v4u a[8];
#pragma unroll
            for (int m = 0; m < 8; ++m)
                a[m] = __builtin_amdgcn_raw_buffer_load_b128(wsr, WS_PART + ((bid - s + m) * 4 + w) * 8192 + s * 1024 + lane * 16, 0, 16);
            if (s == 0 && j == 0 && w == 0 && lane == 0) __hip_atomic_store((gu64*)(ws + WS_EPOCH + p * 128), tag, RLX_AGENT);
            float r[8];
#pragma unroll
            for (int i = 0; i < 8; ++i) r[i] = 0.f;
#pragma unroll
            for (int m = 0; m < 8; ++m)
#pragma unroll
                for (int i = 0; i < 4; ++i) { r[2 * i] += __uint_as_float(a[m][i] << 16); r[2 * i + 1] += __uint_as_float(a[m][i] & 0xffff0000u); }
            u16* ob = out + (size_t)((2 * p + bi) * 48 + l15) * DM + 160 * j + 4 * g;
#pragma unroll
            for (int e = 0; e < 2; ++e) {
                const int t = 2 * s + e;
                if (t < 15) {
                    const int q = t / 5, nt = t - 5 * q;
                    const int c0 = nt < 4 ? 64 * hf + 16 * nt : 128 + 16 * hf;
                    const int col = (j & 1) ? (c0 < 128 ? c0 + 32 : c0 - 128) : c0;
                    *(GAS v2u*)(ob + q * 16 * DM + col) = v2u{pk2(r[4 * e], r[4 * e + 1]), pk2(r[4 * e + 2], r[4 * e + 3])};
                }
            }
        }
    } else {
        constexpr int P1 = 9, P2 = 8;
        const int lw = w - 4, par = lw & 1;
        const int r8 = lane >> 3;
        const unsigned c_row = ((lane & 7) ^ ((par << 2) | (r8 >> 1))) << 4;
        const unsigned c_tr = ((lane & 7) ^ ((((r8 >> 1) & 1) << 1) | (par << 2))) << 4;
        unsigned o_wq[2][3], o_x[3], o_f[2][2], o_v[3];
#pragma unroll
        for (int i = 0; i < 2; ++i) {
#pragma unroll
            for (int cb = 0; cb < 3; ++cb) o_wq[i][cb] = (r8 + 8 * lw + 32 * i) * 12288 + c_tr + cb * 128 + 4096 - cb * 1024;
#pragma unroll
            for (int cb = 0; cb < 2; ++cb) o_f[i][cb] = (r8 + 8 * lw + 32 * i) * 1280 + c_tr + cb * 128 + 4096 - cb * 1024;
        }
#pragma unroll
        for (int i = 0; i < 3; ++i) {
            const int imm = i == 0 ? -4096 : 0, rg = lw + 4 * i;
            o_x[i] = (r8 + 8 * rg) * 1280 + c_row + 4096 - imm;
            o_v[i] = (r8 + 8 * rg + (rg >= 6 ? 32 * 48 - 48 : 0)) * 384 + c_row + 4096 - imm;
        }
        const unsigned o_h = ((lane >> 2) + 16 * lw) * 1280 + (((lane & 3) ^ (((lane >> 5) & 1) << 1)) << 4) + 4096;
        const char* wq_b = (const char*)wq + h * 384 - 4096;
        const char* x_b = (const char*)x + (size_t)(96 * p) * 1280 - 4096;
        int kk = (p + 3 * j) % 10;
        auto rows3 = [&](const char* b, const unsigned* o, unsigned lds) {
            set_m0(lds + 4096);
            dma_imm<-4096>(b, o[0]); dma_imm<0>(b, o[1]);
            set_m0(lds + 8192);
            dma_imm<0>(b, o[2]);
        };
        const char* na; const char* nb; unsigned nsl;
        auto prep1 = [&](int n) {
            na = wq_b + (size_t)kk * (64 * 12288); nb = x_b + kk * 128; nsl = (n & 3) * SLOT;
            kk = kk == 9 ? 0 : kk + 1;
            asm volatile("" : "+s"(na), "+s"(nb), "+s"(nsl));
        };
        auto issue1 = [&]() {
            const unsigned sl = nsl + lw * 3072;
#pragma unroll
            for (int i = 0; i < 2; ++i) {
                set_m0(sl + i * 12288);
                dma_imm<0>(na, o_wq[i][0]); dma_imm<1024>(na, o_wq[i][1]); dma_imm<2048>(na, o_wq[i][2]);
            }
            rows3(nb, o_x, nsl + X_OFF + lw * 1024);
        };
        prep1(0); issue1(); prep1(1); issue1();
        prep1(2);
        wait_vm<P1>();
        BARRIER();
        issue1();
        prep1(3);
        wait_vm<P1>();
        BARRIER();
#pragma unroll 1
        for (int n = 3; n <= 9; ++n) {
            issue1();
            prep1(n + 1);
            wait_vm<P1>();
            BARRIER();
        }
        const char* wo_b = (const char*)wo + j * 320 + ((j & 1) ? 64 : 0) - 4096;
        const int hoff = (j & 1) ? -64 : 256;
        const char* v_b = (const char*)vc + (size_t)((2 * p) * 32 + 4 * s) * (48 * 384) - 4096;
        auto prep2 = [&](int n) {
            const int t = n - 12, t3 = (t * 11) >> 5;
            const int hp = (t3 + hrot) & 3;
            int sub = t - 3 * t3 + (p >> 2); if (sub >= 3) sub -= 3;
            na = wo_b + (size_t)((4 * s + hp) * 192 + sub * 64) * 1280;
            nb = v_b + (size_t)hp * (48 * 384) + sub * 128;
            nsl = (n & 3) * SLOT;
            asm volatile("" : "+s"(na), "+s"(nb), "+s"(nsl));
        };
        auto issue2 = [&]() {
            const unsigned sl = nsl + lw * 2048, s1 = nsl + lw * 1024;
#pragma unroll
            for (int i = 0; i < 2; ++i) {
                set_m0(sl + i * 8192);
                dma_imm<0>(na, o_f[i][0]); dma_imm<1024>(na, o_f[i][1]);
            }
            set_m0(s1 + H_OFF);
            dma_imm<0>(na + hoff, o_h);
            rows3(nb, o_v, s1 + V_OFF);
        };
        {
            const char* kb = (const char*)kc + (size_t)((2 * p) * 32 + h) * (48 * 384) - 4096;
#pragma unroll
            for (int i = 0; i < 3; ++i) {
                const unsigned o = o_v[i] + (i == 0 ? -4096 : 0);
                set_m0(2 * SLOT + lw * 3072 + i * 12288);
                dma_imm<0>(kb, o); dma_imm<1024>(kb, o + 128 - 1024); dma_imm<2048>(kb, o + 256 - 2048);
            }
            prep2(12);
            wait_vm<P1>();
            BARRIER();
        }
        BARRIER();
        issue2(); prep2(13);
        wait_vm<P2>();
        BARRIER();
        issue2(); prep2(14);
        BARRIER();
        issue2(); prep2(15);
        wait_vm<2 * P2>();
        BARRIER();
        issue2(); prep2(16);
        wait_vm<2 * P2>();
        BARRIER();
        wait_vm<P2>();
        BARRIER();
#pragma unroll 1
        for (int n = 16; n <= 21; ++n) {
            issue2();
            prep2(n + 1);
            wait_vm<P2>();
            BARRIER();
        }
        const gu64* pfp = (const gu64*)(ws + WS_FLAGP) + (size_t)((p * 32 + ((lane & 7) >> 1) * 8 + s) * 4 + 2 * (lw & 1) + (lane & 1)) * 16;
        const u64 tag = *(volatile LAS u64*)(uintptr_t)L_TAG;
        u64 pflag;
        issue2(); prep2(23);
        asm volatile("global_load_dwordx2 %0, %1, off sc1" : "=v"(pflag) : "v"(pfp) : "memory");
        wait_vm<P2 + 1>();
        BARRIER();
        issue2();
        asm volatile("s_waitcnt vmcnt(%1)" : "+v"(pflag) : "n"(P2) : "memory");
        BARRIER();
        if (lw >= 2) {
            const int bi = lw - 2;
            const __amdgpu_buffer_rsrc_t wsr = __builtin_amdgcn_make_buffer_rsrc(ws, 0, WS_END, 0x00020000);
            if (!__all(pflag == tag)) {
                for (unsigned spins = 0; spins < SPIN_CAP; ++spins) {
                    if (__all(__hip_atomic_load((gu64*)pfp, RLX_AGENT) == tag)) break;
                    __builtin_amdgcn_s_sleep(1);
                }
            }
            __builtin_amdgcn_fence(__ATOMIC_ACQUIRE, "wavefront");
            v4u p32[4][3]; v2u p16[4][3];
#pragma unroll
            for (int i = 0; i < 4; ++i)
#pragma unroll
                for (int q = 0; q < 3; ++q) {
                    const int sb = ((p * 32 + ((i + hrot) & 3) * 8 + s) * 2 + bi) * 3 + q;
                    p32[i][q] = __builtin_amdgcn_raw_buffer_load_b128(wsr, WS_P32 + sb * 1024 + lane * 16, 0, 16);
                    p16[i][q] = __builtin_amdgcn_raw_buffer_load_b64(wsr, WS_P16 + sb * 512 + lane * 8, 0, 16);
                }
            SBAR();
            asm volatile("s_waitcnt vmcnt(24)" ::: "memory");
            RAW_BARRIER();
            RAW_BARRIER();
            LAS char* P = (LAS char*)smem + bi * 18432;
#pragma unroll
            for (int i = 0; i < 4; ++i)
#pragma unroll
                for (int q = 0; q < 3; ++q) {
                    *(LAS v4u*)(P + (i * 3 + q) * 1024 + lane * 16) = p32[i][q];
                    *(LAS v2u*)(P + 12288 + (i * 3 + q) * 512 + lane * 8) = p16[i][q];
                }
        } else {
            wait_vm<0>();
            BARRIER();
            BARRIER();
        }
        BARRIER();
    }
}

extern "C" void kernel_launch(void* const* d_in, const int* in_sizes, int n_in, void* d_out, int out_size, void* d_ws, size_t ws_size,
                              hipStream_t stream) {
    (void)in_sizes; (void)n_in; (void)out_size; (void)ws_size;
    (void)hipFuncSetAttribute((const void*)mha_fused, hipFuncAttributeMaxDynamicSharedMemorySize, LDS_BYTES);
    mha_fused<<<256, NTHR, LDS_BYTES, stream>>>((const u16*)d_in[0], (const u16*)d_in[1], (const u16*)d_in[2], (const u16*)d_in[3],
                                                (const u16*)d_in[4], (u16*)d_out, (char*)d_ws);
}
```
